```python
import torch

def _softmax(x, axis=-1):
    return torch.softmax(x, dim=axis)
def _gelu(x):
    return torch.nn.functional.gelu(x, approximate="tanh")
def _silu(x):
    return torch.nn.functional.silu(x)
def _sigmoid(x):
    return torch.sigmoid(x)
def _layernorm(x, axis=-1, eps=1e-5):
    m = x.mean(dim=axis, keepdim=True)
    v = x.var(dim=axis, keepdim=True, unbiased=False)
    return (x - m) / torch.sqrt(v + eps)
def _scatter_add(tgt, idx, src, axis=0):
    return tgt.clone().index_add_(axis, idx, src)
def _segment_sum(src, seg, n):
    out = torch.zeros((n,) + src.shape[1:], dtype=src.dtype)
    return out.index_add_(0, seg, src)
def _pad(x, pw):
    flat = tuple(v for pair in reversed(pw) for v in pair)
    return torch.nn.functional.pad(x, flat)

kernel_name = 'concat_heads__spectral_fnet__ssm_conv_h3__prompt'

def setup_inputs():

    _w = (lambda a: (lambda t: t.to(torch.bfloat16) if t.is_floating_point() else t)(torch.as_tensor(a)))
    def _A_setup():
        B, S, D = 1, 2048, 640
        ns = torch.arange(S)[:, None] * torch.arange(S)[None, :]
        nd = torch.arange(D)[:, None] * torch.arange(D)[None, :]
        Ws_re = torch.cos(-2.0 * torch.pi * ns / S).to(float)
        Ws_im = torch.sin(-2.0 * torch.pi * ns / S).to(float)
        Wd_re = torch.cos(-2.0 * torch.pi * nd / D).to(float)
        Wd_im = torch.sin(-2.0 * torch.pi * nd / D).to(float)
        return {
            "x": _w((lambda *s: torch.randn(*s, dtype=torch.bfloat16) * 0.05)(B, S, D)),
            "dft_seq_re": _w(Ws_re),
            "dft_seq_im": _w(Ws_im),
            "dft_hid_re": _w(Wd_re),
            "dft_hid_im": _w(Wd_im),
        }
    _A_in = _A_setup()

    def _B_setup():
        B, S, D = 1, 2048, 640
        ks = 4
        return {
            "q": _w((lambda *s: torch.randn(*s, dtype=torch.bfloat16) * 0.05)(B, S, D)),
            "k": _w((lambda *s: torch.randn(*s, dtype=torch.bfloat16) * 3.2)(B, S, D)),
            "v": _w((lambda *s: torch.randn(*s, dtype=torch.bfloat16) * 3.2)(B, S, D)),
            "shift_k": _w((lambda *s: torch.randn(*s, dtype=torch.bfloat16) * 3.2)(D, ks) * 0.5),
            "ssm_k": _w((lambda *s: torch.randn(*s, dtype=torch.bfloat16) * 3.2)(D, S) * (1.0 / S ** 0.5)),
            "ssm_D": _w((lambda *s: torch.randn(*s, dtype=torch.bfloat16) * 3.2)(D)),
        }
    _B_in = _B_setup()

    return {
        "A_x": _A_in["x"],
        "A_dft_seq_re": _A_in["dft_seq_re"],
        "A_dft_seq_im": _A_in["dft_seq_im"],
        "A_dft_hid_re": _A_in["dft_hid_re"],
        "A_dft_hid_im": _A_in["dft_hid_im"],
        "B_q": _B_in["q"],
        "B_k": _B_in["k"],
        "B_v": _B_in["v"],
        "B_shift_k": _B_in["shift_k"],
        "B_ssm_k": _B_in["ssm_k"],
        "B_ssm_D": _B_in["ssm_D"],
    }

def reference(A_x, A_dft_seq_re, A_dft_seq_im, A_dft_hid_re, A_dft_hid_im, B_q, B_k, B_v, B_shift_k, B_ssm_k, B_ssm_D):
    def _A_ref(x, dft_seq_re, dft_seq_im, dft_hid_re, dft_hid_im):
        a = torch.einsum("bsd,ed->bse", x, dft_hid_re)
        b = torch.einsum("bsd,ed->bse", x, dft_hid_im)
        y = torch.einsum("ts,bse->bte", dft_seq_re, a) - torch.einsum("ts,bse->bte", dft_seq_im, b)
        return y

    def _B_ref(q, k, v, shift_k, ssm_k, ssm_D):
        B, S, D = q.shape
        ks = shift_k.shape[-1]
        k_pad = _pad(k, ((0, 0), (ks - 1, 0), (0, 0)))
        k_shift = torch.zeros_like(k)
        for t in range(ks):
            k_shift = k_shift + k_pad[:, t:t + S, :] * shift_k[:, ks - 1 - t][None, None, :]
        kv = k_shift * v
        L = 2 * S
        kv_pad = _pad(kv, ((0, 0), (0, S), (0, 0)))
        kf_pad = _pad(ssm_k, ((0, 0), (0, S)))
        kv_f = (lambda x, axes=(-2,-1): torch.fft.rfft2(x.float(), dim=axes))(kv_pad[:, None, :, :], axes=(1, 2))
        kf_f = (lambda x, axes=(-2,-1): torch.fft.rfft2(x.float(), dim=axes))(kf_pad[:, None, :], axes=(1, 2))
        kf_f = (lambda x, *d: x.permute(*d))(kf_f, 1, 2, 0)
        y_f = kv_f * kf_f[None, :, :, :]
        y = (lambda x, s, axes=(-2,-1): torch.fft.irfft2(x, s=s, dim=axes).to(torch.bfloat16))(y_f, (1, L), axes=(1, 2))[:, 0, :S, :]
        y = y + kv * ssm_D[None, None, :]
        return q * y

    a = _A_ref(A_x, A_dft_seq_re, A_dft_seq_im, A_dft_hid_re, A_dft_hid_im)
    b = _B_ref(B_q, B_k, B_v, B_shift_k, B_ssm_k, B_ssm_D)
    return torch.concatenate([a, b], axis=-1)
```

```cpp
#include <hip/hip_runtime.h>
#include <hip/hip_bf16.h>
typedef unsigned short u16;
__device__ __forceinline__ float bf2f(u16 v){ return __uint_as_float(((unsigned)v)<<16); }
__device__ __forceinline__ u16 f2bf(float f){ __hip_bfloat16 h = __float2bfloat16(f); return *reinterpret_cast<u16*>(&h); }
__device__ __forceinline__ float rbf(float f){ return bf2f(f2bf(f)); }
constexpr int S=2048, D=640;

__global__ void k_pq(const u16* Cr, const u16* Ci, const u16* x, u16* PQ){
  int d = blockIdx.x*64 + threadIdx.x; int m0 = blockIdx.y*16 + threadIdx.y*4;
  const u16* C = (m0 < S) ? Cr + (size_t)m0*S : Ci + (size_t)(m0-S)*S;
  float a0=0,a1=0,a2=0,a3=0;
  for(int s=0;s<S;++s){ float xv=bf2f(x[s*D+d]);
    a0+=bf2f(C[s])*xv; a1+=bf2f(C[S+s])*xv; a2+=bf2f(C[2*S+s])*xv; a3+=bf2f(C[3*S+s])*xv; }
  PQ[(size_t)(m0+0)*D+d]=f2bf(a0); PQ[(size_t)(m0+1)*D+d]=f2bf(a1); PQ[(size_t)(m0+2)*D+d]=f2bf(a2); PQ[(size_t)(m0+3)*D+d]=f2bf(a3);
}
__global__ void k_y(const u16* PQ, const u16* Wr, const u16* Wi, u16* out){
  int e = blockIdx.x*64 + threadIdx.x; int t = blockIdx.y*4 + threadIdx.y;
  float a=0;
  for(int d=0;d<D;++d){ a += bf2f(PQ[(size_t)t*D+d])*bf2f(Wr[e*D+d]) - bf2f(PQ[(size_t)(S+t)*D+d])*bf2f(Wi[e*D+d]); }
  out[(size_t)t*2*D+e]=f2bf(a);
}
__global__ void k_kv(const u16* k, const u16* v, const u16* sk, u16* kvT){
  int d = blockIdx.x*64 + threadIdx.x; int s = blockIdx.y*4 + threadIdx.y;
  float acc=0;
  for(int t=0;t<4;++t){ int ss=s+t-3; float kv_= ss>=0? bf2f(k[ss*D+d]):0.f; acc = rbf(acc + rbf(kv_*bf2f(sk[d*4+3-t]))); }
  kvT[(size_t)d*S+s]=f2bf(acc*bf2f(v[s*D+d]));
}
__global__ void k_conv(const u16* kvT, const u16* h, const u16* Dv, const u16* q, u16* out){
  __shared__ float su[S], sh[S];
  int d=blockIdx.x;
  for(int i=threadIdx.x;i<S;i+=256){ su[i]=bf2f(kvT[(size_t)d*S+i]); sh[i]=bf2f(h[(size_t)d*S+i]); }
  __syncthreads();
  for(int t=threadIdx.x;t<S;t+=256){
    float a=0; for(int s=0;s<=t;++s) a+=sh[t-s]*su[s];
    float y=rbf(a); y=rbf(y+rbf(su[t]*bf2f(Dv[d])));
    out[(size_t)t*2*D+D+d]=f2bf(bf2f(q[t*D+d])*y);
  }
}
extern "C" void kernel_launch(void* const* d_in, const int* in_sizes, int n_in, void* d_out, int out_size, void* d_ws, size_t ws_size, hipStream_t stream){
  const u16* x=(const u16*)d_in[0]; const u16* Cr=(const u16*)d_in[1]; const u16* Ci=(const u16*)d_in[2];
  const u16* Wr=(const u16*)d_in[3]; const u16* Wi=(const u16*)d_in[4];
  const u16* q=(const u16*)d_in[5]; const u16* k=(const u16*)d_in[6]; const u16* v=(const u16*)d_in[7];
  const u16* sk=(const u16*)d_in[8]; const u16* h=(const u16*)d_in[9]; const u16* Dv=(const u16*)d_in[10];
  u16* out=(u16*)d_out; u16* PQ=(u16*)d_ws; u16* kvT=PQ+(size_t)2*S*D;
  k_pq<<<dim3(D/64, 2*S/16), dim3(64,4),0,stream>>>(Cr,Ci,x,PQ);
  k_y<<<dim3(D/64,S/4),dim3(64,4),0,stream>>>(PQ,Wr,Wi,out);
  k_kv<<<dim3(D/64,S/4),dim3(64,4),0,stream>>>(k,v,sk,kvT);
  k_conv<<<D,256,0,stream>>>(kvT,h,Dv,q,out);
}
```

```cpp
#include <hip/hip_runtime.h>
#include <hip/hip_bf16.h>
#include <utility>
#define NTA 0
#define PF 1
typedef unsigned short u16;
typedef __attribute__((ext_vector_type(4))) unsigned u32x4;
typedef __attribute__((ext_vector_type(2))) unsigned u32x2;
typedef __attribute__((ext_vector_type(16))) float f32x16;
typedef __attribute__((ext_vector_type(4))) float f32x4;
#define DI __device__ __forceinline__
template<int B,int E,class F> DI void sfor(F&& f){ if constexpr(B<E){ f(std::integral_constant<int,B>{}); sfor<B+1,E>(f);} }
template<int OFF,int NT> DI void glds(unsigned voff,const char* p){
  if constexpr(NT) asm volatile("global_load_lds_dwordx4 %0, %1 offset:%2 nt"::"v"(voff),"s"(p),"i"(OFF):"memory");
  else             asm volatile("global_load_lds_dwordx4 %0, %1 offset:%2"::"v"(voff),"s"(p),"i"(OFF):"memory");
}
DI void setm0(unsigned v){ asm volatile("s_mov_b32 m0, %0\n\ts_nop 0"::"s"(v):"memory"); }
template<int OFF> DI void rd128(u32x4& r,unsigned a){ asm volatile("ds_read_b128 %0, %1 offset:%2":"=v"(r):"v"(a),"i"(OFF):"memory"); }
template<int OFF> DI void rdtr(u32x2& r,unsigned a){ asm volatile("ds_read_b64_tr_b16 %0, %1 offset:%2":"=v"(r):"v"(a),"i"(OFF):"memory"); }
DI void mf32(f32x16& c,const u32x4& a,const u32x4& b){ asm volatile("v_mfma_f32_32x32x16_bf16 %0, %1, %2, %0":"+v"(c):"v"(a),"v"(b)); }
DI void mf16(f32x4& c,const u32x4& a,const u32x4& b){ asm volatile("v_mfma_f32_16x16x32_bf16 %0, %1, %2, %0":"+v"(c):"v"(a),"v"(b)); }
template<int N> DI void wait_vm(){ asm volatile("s_waitcnt vmcnt(%0)"::"i"(N):"memory"); }
template<int N> DI void wait_lgkm(){ asm volatile("s_waitcnt lgkmcnt(%0)"::"i"(N):"memory"); }
DI void bar(){ asm volatile("s_barrier":::"memory"); }
DI unsigned cvtpk(float lo,float hi){ unsigned r; asm("v_cvt_pk_bf16_f32 %0, %1, %2":"=v"(r):"v"(lo),"v"(hi)); return r; }
DI float bf2f(u16 v){ return __uint_as_float(((unsigned)v)<<16); }
DI float bflo(unsigned v){ return __uint_as_float(v<<16); }
DI float bfhi(unsigned v){ return __uint_as_float(v&0xffff0000u); }
constexpr int S=2048, D=640;
constexpr int NS=4, SLOT=32768;
struct Frag { u32x4 x[2]; u32x4 c[2]; };

__global__ __launch_bounds__(512,2) void k1(const u16* __restrict__ Cr,const u16* __restrict__ Ci,const u16* __restrict__ x,u16* __restrict__ PQ){
  extern __shared__ __attribute__((aligned(16))) char smem[];
  const int tid=threadIdx.x, lane=tid&63, wid=__builtin_amdgcn_readfirstlane(tid>>6);
  const int xcd=blockIdx.x&7, loc=blockIdx.x>>3;
  const int rot = xcd*4;
  if(loc<20){
    const int i = xcd*4 + loc/5, j = loc%5;
    if(wid>=4){
      const int L=wid-4;
      const char* cb = (const char*)((L>>1)? Ci : Cr) + (size_t)(i*64 + (L&1)*32)*4096;
      const char* xb = (const char*)x + j*256 + (size_t)(L*16)*1280;
      unsigned vc[4], vx[4];
      #pragma unroll
      for(int p=0;p<4;++p){
        int row=8*p+(lane>>3); int sw=((p&1)*4+(lane>>4))&7;
        vc[p]=row*4096 + (((lane&7)^sw)<<4) - 1024*p;
        int xr=4*p+(lane>>4);
        vx[p]=xr*1280 + (((lane&15)^((lane>>4)<<2))<<4) - 1024*p;
      }
      auto issue=[&](int st){
        int ks=(st+rot)&31;
        unsigned slot=(st&(NS-1))*SLOT;
        const char* c=cb+ks*128; const char* xx=xb+(size_t)ks*64*1280;
        setm0(slot+L*4096);
        glds<0,NTA>(vc[0],c); glds<1024,NTA>(vc[1],c); glds<2048,NTA>(vc[2],c); glds<3072,NTA>(vc[3],c);
        setm0(slot+16384+L*4096);
        glds<0,0>(vx[0],xx); glds<1024,0>(vx[1],xx); glds<2048,0>(vx[2],xx); glds<3072,0>(vx[3],xx);
      };
      issue(0); issue(1); issue(2);
      wait_vm<16>(); bar();
      for(int s=0;s<29;++s){ issue(s+3); wait_vm<16>(); bar(); }
      wait_vm<8>(); bar();
      wait_vm<0>(); bar();
    } else {
      const int wd=wid&1, wp=wid>>1;
      const int h=lane>>5, r=lane&31, q=(lane>>2)&3, p=lane&3, blk=(lane>>4)&1;
      unsigned ax[2], ac[4];
      #pragma unroll
      for(int dt=0;dt<2;++dt) ax[dt]=16384 + (8*h+q)*256 + (((2*wd+dt)^q)<<6) + blk*32 + p*8;
      #pragma unroll
      for(int ks=0;ks<4;++ks) ac[ks]=(64*wp+r)*128 + (((2*ks+h)^((r>>1)&7))<<4);
      f32x16 acc[2][2];
      #pragma unroll
      for(int a=0;a<2;++a)
        #pragma unroll
        for(int b=0;b<2;++b)
          #pragma unroll
          for(int e=0;e<16;++e) acc[a][b][e]=0.f;
      Frag F0,F1;
      auto rdx=[&](Frag& F,auto KS,auto DT){ constexpr int ks=KS.value, dt=DT.value;
        u32x2 lo,hi; rdtr<ks*4096>(lo,ax[dt]); rdtr<ks*4096+1024>(hi,ax[dt]);
        F.x[dt]=u32x4{lo[0],lo[1],hi[0],hi[1]}; };
      auto rdc=[&](Frag& F,auto KS,auto TT){ constexpr int ks=KS.value, tt=TT.value; rd128<tt*4096>(F.c[tt],ac[ks]); };
      using I0=std::integral_constant<int,0>; using I1=std::integral_constant<int,1>; using I2=std::integral_constant<int,2>; using I3=std::integral_constant<int,3>;
      auto step=[&](Frag& Fc,Frag& Fn,auto KN){
        wait_lgkm<0>();
        mf32(acc[0][0],Fc.x[0],Fc.c[0]);
        rdx(Fn,KN,I0{}); rdc(Fn,KN,I0{});
        mf32(acc[0][1],Fc.x[0],Fc.c[1]);
        rdx(Fn,KN,I1{}); rdc(Fn,KN,I1{});
        mf32(acc[1][0],Fc.x[1],Fc.c[0]);
        mf32(acc[1][1],Fc.x[1],Fc.c[1]);
      };
      bar();
      rdx(F0,I0{},I0{}); rdc(F0,I0{},I0{}); rdx(F0,I0{},I1{}); rdc(F0,I0{},I1{});
      for(int s=0;s<32;++s){
        step(F0,F1,I1{});
        step(F1,F0,I2{});
        step(F0,F1,I3{});
        wait_lgkm<0>();
        mf32(acc[0][0],F1.x[0],F1.c[0]);
        if(s<31){
          unsigned m = (s&1)? 0x18000u : 0x8000u;
          #pragma unroll
          for(int dt=0;dt<2;++dt) ax[dt]^=m;
          #pragma unroll
          for(int ks=0;ks<4;++ks) ac[ks]^=m;
          bar();
          rdx(F0,I0{},I0{}); rdc(F0,I0{},I0{});
          mf32(acc[0][1],F1.x[0],F1.c[1]);
          rdx(F0,I0{},I1{}); rdc(F0,I0{},I1{});
        } else {
          mf32(acc[0][1],F1.x[0],F1.c[1]);
        }
        mf32(acc[1][0],F1.x[1],F1.c[0]);
        mf32(acc[1][1],F1.x[1],F1.c[1]);
      }
      asm volatile("s_nop 15\n\ts_nop 3":"+v"(acc[0][0]),"+v"(acc[0][1]),"+v"(acc[1][0]),"+v"(acc[1][1]));
      const float sg = wp? -1.f : 1.f;
      char* pq=(char*)PQ + (size_t)((i*5+j)*2+wp)*16384;
      #pragma unroll
      for(int dt=0;dt<2;++dt)
        #pragma unroll
        for(int tt=0;tt<2;++tt)
          #pragma unroll
          for(int s2=0;s2<2;++s2){
            const f32x16& a=acc[dt][tt];
            unsigned g0x=cvtpk(sg*a[8*s2+0],sg*a[8*s2+1]), g0y=cvtpk(sg*a[8*s2+2],sg*a[8*s2+3]);
            unsigned g1x=cvtpk(sg*a[8*s2+4],sg*a[8*s2+5]), g1y=cvtpk(sg*a[8*s2+6],sg*a[8*s2+7]);
            auto rx=__builtin_amdgcn_permlane32_swap(g0x,g1x,false,false);
            auto ry=__builtin_amdgcn_permlane32_swap(g0y,g1y,false,false);
            u32x4 o={rx[0],ry[0],rx[1],ry[1]};
            int dblk=wd*4+dt*2+s2;
            *(u32x4*)(pq + (dblk*2+tt)*1024 + lane*16)=o;
          }
    }
  } else if(PF){
    const int hw=(loc-20)*8+wid, nhw=12*8;
    setm0(wid*256);
    for(int id=hw; id<32*18; id+=nhw){
      int st=id/18, n=id%18; int ks=(st+rot)&31;
      if(n<8){ const char* p=(const char*)((n&1)?Ci:Cr)+(size_t)(xcd*256+(n>>1)*64)*4096+ks*128; unsigned vo=lane*4096;
        asm volatile("global_load_lds_dword %0, %1"::"v"(vo),"s"(p):"memory");
      } else { const char* p=(const char*)x+(size_t)ks*81920+(n-8)*8192; unsigned vo=lane*128;
        asm volatile("global_load_lds_dword %0, %1"::"v"(vo),"s"(p):"memory"); }
    }
    wait_vm<0>();
  }
}

typedef __attribute__((ext_vector_type(8))) short bf16x8;
__global__ __launch_bounds__(256) void k2(const u16* __restrict__ PQ,const u16* __restrict__ Wr,const u16* __restrict__ Wi,u16* __restrict__ out){
  const int lane=threadIdx.x&63, wid=threadIdx.x>>6;
  const int xcd=blockIdx.x&7, loc=blockIdx.x>>3; const int i=xcd*4+loc/5, jp=loc%5;
  const int e=jp*128+wid*32+(lane&31), h=lane>>5;
  f32x16 acc[2]; for(int a=0;a<2;++a) for(int k=0;k<16;++k) acc[a][k]=0.f;
  for(int j=0;j<5;++j) for(int pt=0;pt<2;++pt){
    const u16* W=(pt?Wi:Wr)+(size_t)e*D+j*128+h*8;
    const char* pq=(const char*)PQ+(size_t)((i*5+j)*2+pt)*16384+lane*16;
    #pragma unroll
    for(int db=0;db<8;++db){
      bf16x8 w=*(const bf16x8*)(W+db*16);
      bf16x8 p0=*(const bf16x8*)(pq+(db*2+0)*1024), p1=*(const bf16x8*)(pq+(db*2+1)*1024);
      acc[0]=__builtin_amdgcn_mfma_f32_32x32x16_bf16(w,p0,acc[0],0,0,0);
      acc[1]=__builtin_amdgcn_mfma_f32_32x32x16_bf16(w,p1,acc[1],0,0,0);
    }
  }
  #pragma unroll
  for(int tt=0;tt<2;++tt){
    int t=i*64+tt*32+(lane&31);
    #pragma unroll
    for(int g=0;g<4;++g){
      u32x2 o={cvtpk(acc[tt][4*g],acc[tt][4*g+1]),cvtpk(acc[tt][4*g+2],acc[tt][4*g+3])};
      *(u32x2*)(out+(size_t)t*2*D+jp*128+wid*32+8*g+4*h)=o;
    }
  }
}

__global__ __launch_bounds__(256) void kprep(const u16* __restrict__ k,const u16* __restrict__ v,const u16* __restrict__ sk,u16* __restrict__ R){
  const int c2=threadIdx.x&31, tg=threadIdx.x>>5;
  const int d=(blockIdx.x%10)*64+c2*2, s0=(blockIdx.x/10)*64+tg*8;
  u32x4 w=*(const u32x4*)(sk+d*4);
  float wa[4]={bflo(w[0]),bfhi(w[0]),bflo(w[1]),bfhi(w[1])}, wb[4]={bflo(w[2]),bfhi(w[2]),bflo(w[3]),bfhi(w[3])};
  unsigned kk[11], vv[8];
  #pragma unroll
  for(int r=0;r<11;++r){ int s=s0-3+r; kk[r]= s>=0? *(const unsigned*)(k+(size_t)s*D+d):0u; }
  #pragma unroll
  for(int r=0;r<8;++r) vv[r]=*(const unsigned*)(v+(size_t)(s0+r)*D+d);
  float ua[8],ub[8];
  #pragma unroll
  for(int r=0;r<8;++r){
    float a=bflo(kk[r])*wa[3]; a=fmaf(bflo(kk[r+1]),wa[2],a); a=fmaf(bflo(kk[r+2]),wa[1],a); a=fmaf(bflo(kk[r+3]),wa[0],a);
    float b=bfhi(kk[r])*wb[3]; b=fmaf(bfhi(kk[r+1]),wb[2],b); b=fmaf(bfhi(kk[r+2]),wb[1],b); b=fmaf(bfhi(kk[r+3]),wb[0],b);
    ua[r]=a*bflo(vv[r]); ub[r]=b*bfhi(vv[r]);
  }
  u32x4 oa={cvtpk(ua[7],ua[6]),cvtpk(ua[5],ua[4]),cvtpk(ua[3],ua[2]),cvtpk(ua[1],ua[0])};
  u32x4 ob={cvtpk(ub[7],ub[6]),cvtpk(ub[5],ub[4]),cvtpk(ub[3],ub[2]),cvtpk(ub[1],ub[0])};
  *(u32x4*)(R+(size_t)d*S+(2040-s0))=oa;
  *(u32x4*)(R+(size_t)(d+1)*S+(2040-s0))=ob;
}

constexpr int HCS=4160, RCS=4576, CHB=4*HCS+2*RCS;
__global__ __launch_bounds__(256) void kconv(const u16* __restrict__ R,const u16* __restrict__ hk,const u16* __restrict__ Dv,u16* __restrict__ yT){
  extern __shared__ __attribute__((aligned(16))) char smem[];
  const int lane=threadIdx.x&63, wid=__builtin_amdgcn_readfirstlane(threadIdx.x>>6);
  const int d=blockIdx.x*4+wid;
  char* L=smem+wid*CHB;
  const u16* hp=hk+(size_t)d*S;
  #pragma unroll
  for(int r=0;r<4;++r){
    int ch=r*64+lane;
    u32x4 w=*(const u32x4*)(hp+ch*8);
    u32x2 pv= ch>0? *(const u32x2*)(hp+ch*8-4) : u32x2{0u,0u};
    unsigned om2=__builtin_amdgcn_alignbit(pv[1],pv[0],16), om1=__builtin_amdgcn_alignbit(w[0],pv[1],16);
    unsigned o0=__builtin_amdgcn_alignbit(w[1],w[0],16), o1=__builtin_amdgcn_alignbit(w[2],w[1],16), o2=__builtin_amdgcn_alignbit(w[3],w[2],16);
    char* p=L+(ch+3)*16;
    *(u32x4*)(p+0*HCS)=u32x4{om2,om1,o0,o1};
    *(u32x4*)(p+1*HCS)=u32x4{pv[1],w[0],w[1],w[2]};
    *(u32x4*)(p+2*HCS)=u32x4{om1,o0,o1,o2};
    *(u32x4*)(p+3*HCS)=w;
  }
  if(lane<12) *(u32x4*)(L+(lane&3)*HCS+(lane>>2)*16)=u32x4{0,0,0,0};
  const u16* rp=R+(size_t)d*S;
  char* Lr=L+4*HCS;
  #pragma unroll
  for(int r=0;r<4;++r){
    int ch=r*64+lane;
    u32x4 w=*(const u32x4*)(rp+ch*8);
    *(u32x4*)(Lr+ch*16)=w;
    *(u32x2*)(Lr+RCS+ch*16+8)=u32x2{w[0],w[1]};
    *(u32x2*)(Lr+RCS+ch*16+16)=u32x2{w[2],w[3]};
  }
  if(lane<60) *(u32x2*)(Lr+4096+lane*8)=u32x2{0,0};
  if(lane<59) *(u32x2*)(Lr+RCS+4104+lane*8)=u32x2{0,0};
  const int m=lane&15, g=lane>>4;
  unsigned dv=Dv[d];
  u32x4 AD={0,0,0,0};
  if(g==3-(m>>2)){ int j=3-(m&3); unsigned val=(j&1)? (dv<<16):dv; if(j>>1) AD[1]=val; else AD[0]=val; }
  unsigned lb=(unsigned)(size_t)(L-smem);
  unsigned aa=lb+(m&3)*HCS+16*((m>>2)+g);
  unsigned ab=lb+4*HCS+((m&1)? 2*(2016-1792) : RCS+2*(2024-1792)) - 64*(m>>1) + 16*g;
  f32x4 acc[8];
  #pragma unroll
  for(int n=0;n<8;++n) acc[n]=f32x4{0,0,0,0};
  asm volatile("s_waitcnt lgkmcnt(0) vmcnt(0)":::"memory");
  u32x4 A[8],B[8];
  for(int kap=0;kap<8;++kap){
    sfor<0,8>([&](auto I){ constexpr int i=I.value; rd128<(7-i)*512>(B[i],ab); });
    sfor<0,8>([&](auto I){ constexpr int i=I.value; rd128<i*512>(A[i],aa); });
    wait_lgkm<0>();
    if(kap==0){ sfor<0,8>([&](auto I){ constexpr int i=I.value; mf16(acc[i],AD,B[i]); }); }
    sfor<0,8>([&](auto Lx){ constexpr int l=Lx.value;
      sfor<0,8-l>([&](auto M){ constexpr int mm=M.value; mf16(acc[l+mm],A[l],B[mm]); }); });
    aa+=64; ab+=64;
  }
  asm volatile("s_nop 15":"+v"(acc[0]),"+v"(acc[1]),"+v"(acc[2]),"+v"(acc[3]),"+v"(acc[4]),"+v"(acc[5]),"+v"(acc[6]),"+v"(acc[7]));
  #pragma unroll
  for(int n=0;n<8;++n){
    int t=256*n+8*g+4*(m&1)+32*(m>>1);
    *(u32x2*)(yT+(size_t)d*S+t)=u32x2{cvtpk(acc[n][0],acc[n][1]),cvtpk(acc[n][2],acc[n][3])};
  }
}
__global__ __launch_bounds__(256) void kpost(const u16* __restrict__ yT,const u16* __restrict__ q,u16* __restrict__ out){
  const int c2=threadIdx.x&31, tg=threadIdx.x>>5;
  const int d=(blockIdx.x%10)*64+c2*2, s0=(blockIdx.x/10)*64+tg*8;
  u32x4 a=*(const u32x4*)(yT+(size_t)d*S+s0), b=*(const u32x4*)(yT+(size_t)(d+1)*S+s0);
  #pragma unroll
  for(int r=0;r<8;++r){
    unsigned qq=*(const unsigned*)(q+(size_t)(s0+r)*D+d);
    float ya=(r&1)? bfhi(a[r>>1]):bflo(a[r>>1]), yb=(r&1)? bfhi(b[r>>1]):bflo(b[r>>1]);
    *(unsigned*)(out+(size_t)(s0+r)*2*D+D+d)=cvtpk(ya*bflo(qq),yb*bfhi(qq));
  }
}

extern "C" void kernel_launch(void* const* d_in, const int* in_sizes, int n_in, void* d_out, int out_size, void* d_ws, size_t ws_size, hipStream_t stream){
  const u16* x=(const u16*)d_in[0]; const u16* Cr=(const u16*)d_in[1]; const u16* Ci=(const u16*)d_in[2];
  const u16* Wr=(const u16*)d_in[3]; const u16* Wi=(const u16*)d_in[4];
  const u16* q=(const u16*)d_in[5]; const u16* k=(const u16*)d_in[6]; const u16* v=(const u16*)d_in[7];
  const u16* sk=(const u16*)d_in[8]; const u16* h=(const u16*)d_in[9]; const u16* Dv=(const u16*)d_in[10];
  u16* out=(u16*)d_out; u16* PQ=(u16*)d_ws; u16* R=PQ+(size_t)2*S*D; u16* yT=R+(size_t)S*D;
  (void)hipFuncSetAttribute((const void*)k1,hipFuncAttributeMaxDynamicSharedMemorySize,NS*SLOT);
  (void)hipFuncSetAttribute((const void*)kconv,hipFuncAttributeMaxDynamicSharedMemorySize,4*CHB);
  k1<<<256,512,NS*SLOT,stream>>>(Cr,Ci,x,PQ);
  k2<<<160,256,0,stream>>>(PQ,Wr,Wi,out);
  kprep<<<320,256,0,stream>>>(k,v,sk,R);
  kconv<<<160,256,4*CHB,stream>>>(R,h,Dv,yT);
  kpost<<<320,256,0,stream>>>(yT,q,out);
}
```

```cpp
#include <hip/hip_runtime.h>
#include <hip/hip_bf16.h>
typedef unsigned short u16;
typedef unsigned long long u64;
typedef __attribute__((ext_vector_type(4))) unsigned u32x4;
typedef __attribute__((ext_vector_type(2))) unsigned u32x2;
typedef __attribute__((ext_vector_type(16))) float f32x16;
#define DI __device__ __forceinline__
DI void glds4(unsigned m0v,const unsigned (&v)[4],const char* p){
  asm volatile("s_mov_b32 m0, %5\n\ts_nop 0\n\tglobal_load_lds_dwordx4 %0, %4\n\tglobal_load_lds_dwordx4 %1, %4 offset:1024\n\t"
               "global_load_lds_dwordx4 %2, %4 offset:2048\n\tglobal_load_lds_dwordx4 %3, %4 offset:3072"
    ::"v"(v[0]),"v"(v[1]),"v"(v[2]),"v"(v[3]),"s"(p),"s"(m0v):"memory"); }
DI void glds1(unsigned m0v,unsigned v,const char* p){ asm volatile("s_mov_b32 m0, %2\n\ts_nop 0\n\tglobal_load_lds_dwordx4 %0, %1"::"v"(v),"s"(p),"s"(m0v):"memory"); }
template<int N> DI void wait_vm(){ asm volatile("s_waitcnt vmcnt(%0)"::"i"(N):"memory"); }
DI void bar(){ asm volatile("s_barrier":::"memory"); }
DI unsigned cvtpk(float lo,float hi){ unsigned r; asm("v_cvt_pk_bf16_f32 %0, %1, %2":"=v"(r):"v"(lo),"v"(hi)); return r; }
DI float bflo(unsigned v){ return __uint_as_float(v<<16); }
DI float bfhi(unsigned v){ return __uint_as_float(v&0xffff0000u); }
DI float fma1(float a,float b,float c){ float r; asm("v_fma_f32 %0, %1, %2, %3":"=v"(r):"v"(a),"v"(b),"v"(c)); return r; }
DI float mul1(float a,float b){ float r; asm("v_mul_f32 %0, %1, %2":"=v"(r):"v"(a),"v"(b)); return r; }
DI void st16_dev(unsigned vo,const char* p,const u32x4& v){ asm volatile("global_store_dwordx4 %0, %1, %2 sc1\n\ts_nop 1"::"v"(vo),"v"(v),"s"(p):"memory"); }
DI void st_out(u16* p,const u32x4& v){ asm volatile("global_store_dwordx4 %0, %1, off sc1\n\ts_nop 1"::"v"(p),"v"(v):"memory"); }
DI u64 ld8_dev(unsigned vo,const char* p){ u64 v; asm volatile("global_load_dwordx2 %0, %1, %2 sc1\n\ts_waitcnt vmcnt(0)":"=&v"(v):"v"(vo),"s"(p):"memory"); return v; }
DI u64 sld8(const char* p){ u64 v; asm volatile("s_load_dwordx2 %0, %1, 0x0\n\ts_waitcnt lgkmcnt(0)":"=&s"(v):"s"(p):"memory"); return v; }
DI void pf(const char* p){ asm volatile("global_load_dword v126, %0, off"::"v"(p):"memory","v126"); }
DI u32x4 tag4(u64 t){ return u32x4{(unsigned)t,(unsigned)(t>>32),(unsigned)t,(unsigned)(t>>32)}; }

constexpr int S=2048, D=640;
constexpr int SLOT=32768, LDSB=163840;
constexpr int HCS=4160, RCS=4576, HIM=4*HCS, RIM=2*RCS+16;
constexpr int VOFF=33792, ROFF=4*HIM;
constexpr size_t WS_PQ=0, WS_R=WS_PQ+32*163840, WS_Y=WS_R+(size_t)S*D*2, WS_FA=WS_Y+80*64*640, WS_FB=WS_FA+32*8192, WS_EP=WS_FB+10*8192;
constexpr int SPIN=1<<18;
constexpr int W6D=4, CODEW=2, CODEB=21504, S_KV=13, S_ST=20, S_U=24, S_V=27, W_PURE=2, PF_DEPTH=5, RSN=5, RSL=7, POLL_DELAY=16, F_DELAY=0, Y_DELAY=12, EP_AT=1500;
constexpr unsigned PURE_MASK=0x0891E000u, SKIP_MASK=63u, XSKIP=0x7C0u;
constexpr int npf(int a,int b){ unsigned m=~(PURE_MASK|SKIP_MASK)&(unsigned)(((1ull<<b)-1)^((1ull<<a)-1)); return 2*__builtin_popcount(m)-__builtin_popcount(m&XSKIP); }
DI int chunk_of(int j,int n){ int c=j+n; return c>=5? c-5 : c; }
DI int wcol(int j,int st){ return st<2? j*256+st*128 : chunk_of(j,1+((st-2)&3))*256+((st-2)>>2)*128; }

struct Shift { u32x4 a,b,c; };
DI void fill_filter(const Shift (&s)[4],const u32x4 (&hw)[4],char* Lh,int lane){
  #pragma unroll
  for(int r=0;r<4;++r){ char* p=Lh+(r*64+lane+3)*16;
    *(u32x4*)(p+0*HCS)=s[r].a; *(u32x4*)(p+1*HCS)=s[r].b; *(u32x4*)(p+2*HCS)=s[r].c; *(u32x4*)(p+3*HCS)=hw[r]; }
}
DI void conv_chan(unsigned (&y)[16],unsigned himg,unsigned rimg,unsigned dd,int lane){
  const int m=lane&15, g=lane>>4;
  u32x4 AD={0,0,0,0};
  if(g==3-(m>>2)){ int jj=3-(m&3); unsigned val=(jj&1)? (dd<<16):dd; if(jj>>1) AD[1]=val; else AD[0]=val; }
  unsigned aa=himg+(m&3)*HCS+16*((m>>2)+g);
  unsigned ab=rimg+((m&1)? 2*(2016-1792) : RCS+2*(2024-1792)) - 64*(m>>1) + 16*g;
  unsigned scnt=4;
asm volatile(
  "ds_read_b128 v[192:195], %[ab] offset:3584\n\t"
  "ds_read_b128 v[196:199], %[ab] offset:3072\n\t"
  "ds_read_b128 v[200:203], %[ab] offset:2560\n\t"
  "ds_read_b128 v[204:207], %[ab] offset:2048\n\t"
  "ds_read_b128 v[208:211], %[ab] offset:1536\n\t"
  "ds_read_b128 v[212:215], %[ab] offset:1024\n\t"
  "ds_read_b128 v[216:219], %[ab] offset:512\n\t"
  "ds_read_b128 v[220:223], %[ab] offset:0\n\t"
  "ds_read_b128 v[160:163], %[aa] offset:0\n\t"
  "ds_read_b128 v[164:167], %[aa] offset:512\n\t"
  "ds_read_b128 v[168:171], %[aa] offset:1024\n\t"
  "ds_read_b128 v[172:175], %[aa] offset:1536\n\t"
  "s_waitcnt lgkmcnt(4)\n\t"
  "v_mfma_f32_16x16x32_bf16 v[128:131], %[ad], v[192:195], 0\n\t"
  "v_mfma_f32_16x16x32_bf16 v[132:135], %[ad], v[196:199], 0\n\t"
  "v_mfma_f32_16x16x32_bf16 v[136:139], %[ad], v[200:203], 0\n\t"
  "v_mfma_f32_16x16x32_bf16 v[140:143], %[ad], v[204:207], 0\n\t"
  "v_mfma_f32_16x16x32_bf16 v[144:147], %[ad], v[208:211], 0\n\t"
  "v_mfma_f32_16x16x32_bf16 v[148:151], %[ad], v[212:215], 0\n\t"
  "v_mfma_f32_16x16x32_bf16 v[152:155], %[ad], v[216:219], 0\n\t"
  "v_mfma_f32_16x16x32_bf16 v[156:159], %[ad], v[220:223], 0\n\t"
  "s_waitcnt lgkmcnt(0)\n\t"
  "1:\n\t"
  "s_waitcnt lgkmcnt(2)\n\t"
  "v_mfma_f32_16x16x32_bf16 v[128:131], v[160:163], v[192:195], v[128:131]\n\t"
  "v_mfma_f32_16x16x32_bf16 v[132:135], v[160:163], v[196:199], v[132:135]\n\t"
  "ds_read_b128 v[176:179], %[aa] offset:2048\n\t"
  "v_mfma_f32_16x16x32_bf16 v[136:139], v[160:163], v[200:203], v[136:139]\n\t"
  "v_mfma_f32_16x16x32_bf16 v[140:143], v[160:163], v[204:207], v[140:143]\n\t"
  "ds_read_b128 v[180:183], %[aa] offset:2560\n\t"
  "v_mfma_f32_16x16x32_bf16 v[144:147], v[160:163], v[208:211], v[144:147]\n\t"
  "v_mfma_f32_16x16x32_bf16 v[148:151], v[160:163], v[212:215], v[148:151]\n\t"
  "ds_read_b128 v[184:187], %[aa] offset:3072\n\t"
  "v_mfma_f32_16x16x32_bf16 v[152:155], v[160:163], v[216:219], v[152:155]\n\t"
  "s_waitcnt lgkmcnt(3)\n\t"
  "v_mfma_f32_16x16x32_bf16 v[156:159], v[160:163], v[220:223], v[156:159]\n\t"
  "ds_read_b128 v[188:191], %[aa] offset:3584\n\t"
  "v_mfma_f32_16x16x32_bf16 v[132:135], v[164:167], v[192:195], v[132:135]\n\t"
  "v_mfma_f32_16x16x32_bf16 v[136:139], v[164:167], v[196:199], v[136:139]\n\t"
  "ds_read_b128 v[224:227], %[ab] offset:3648\n\t"
  "v_mfma_f32_16x16x32_bf16 v[140:143], v[164:167], v[200:203], v[140:143]\n\t"
  "v_mfma_f32_16x16x32_bf16 v[144:147], v[164:167], v[204:207], v[144:147]\n\t"
  "ds_read_b128 v[160:163], %[aa] offset:64\n\t"
  "v_mfma_f32_16x16x32_bf16 v[148:151], v[164:167], v[208:211], v[148:151]\n\t"
  "v_mfma_f32_16x16x32_bf16 v[152:155], v[164:167], v[212:215], v[152:155]\n\t"
  "ds_read_b128 v[228:231], %[ab] offset:3136\n\t"
  "v_mfma_f32_16x16x32_bf16 v[156:159], v[164:167], v[216:219], v[156:159]\n\t"
  "v_mfma_f32_16x16x32_bf16 v[136:139], v[168:171], v[192:195], v[136:139]\n\t"
  "ds_read_b128 v[232:235], %[ab] offset:2624\n\t"
  "v_mfma_f32_16x16x32_bf16 v[140:143], v[168:171], v[196:199], v[140:143]\n\t"
  "v_mfma_f32_16x16x32_bf16 v[144:147], v[168:171], v[200:203], v[144:147]\n\t"
  "ds_read_b128 v[164:167], %[aa] offset:576\n\t"
  "v_mfma_f32_16x16x32_bf16 v[148:151], v[168:171], v[204:207], v[148:151]\n\t"
  "v_mfma_f32_16x16x32_bf16 v[152:155], v[168:171], v[208:211], v[152:155]\n\t"
  "ds_read_b128 v[236:239], %[ab] offset:2112\n\t"
  "v_mfma_f32_16x16x32_bf16 v[156:159], v[168:171], v[212:215], v[156:159]\n\t"
  "v_mfma_f32_16x16x32_bf16 v[140:143], v[172:175], v[192:195], v[140:143]\n\t"
  "ds_read_b128 v[240:243], %[ab] offset:1600\n\t"
  "v_mfma_f32_16x16x32_bf16 v[144:147], v[172:175], v[196:199], v[144:147]\n\t"
  "v_mfma_f32_16x16x32_bf16 v[148:151], v[172:175], v[200:203], v[148:151]\n\t"
  "ds_read_b128 v[168:171], %[aa] offset:1088\n\t"
  "v_mfma_f32_16x16x32_bf16 v[152:155], v[172:175], v[204:207], v[152:155]\n\t"
  "v_mfma_f32_16x16x32_bf16 v[156:159], v[172:175], v[208:211], v[156:159]\n\t"
  "ds_read_b128 v[244:247], %[ab] offset:1088\n\t"
  "s_waitcnt lgkmcnt(9)\n\t"
  "v_mfma_f32_16x16x32_bf16 v[144:147], v[176:179], v[192:195], v[144:147]\n\t"
  "v_mfma_f32_16x16x32_bf16 v[148:151], v[176:179], v[196:199], v[148:151]\n\t"
  "ds_read_b128 v[248:251], %[ab] offset:576\n\t"
  "v_mfma_f32_16x16x32_bf16 v[152:155], v[176:179], v[200:203], v[152:155]\n\t"
  "v_mfma_f32_16x16x32_bf16 v[156:159], v[176:179], v[204:207], v[156:159]\n\t"
  "ds_read_b128 v[172:175], %[aa] offset:1600\n\t"
  "v_mfma_f32_16x16x32_bf16 v[148:151], v[180:183], v[192:195], v[148:151]\n\t"
  "v_mfma_f32_16x16x32_bf16 v[152:155], v[180:183], v[196:199], v[152:155]\n\t"
  "ds_read_b128 v[252:255], %[ab] offset:64\n\t"
  "v_mfma_f32_16x16x32_bf16 v[156:159], v[180:183], v[200:203], v[156:159]\n\t"
  "s_sub_u32 %[cnt], %[cnt], 1\n\t"
  "v_mfma_f32_16x16x32_bf16 v[152:155], v[184:187], v[192:195], v[152:155]\n\t"
  "s_cmp_lg_u32 %[cnt], 0\n\t"
  "v_mfma_f32_16x16x32_bf16 v[156:159], v[184:187], v[196:199], v[156:159]\n\t"
  "v_mfma_f32_16x16x32_bf16 v[156:159], v[188:191], v[192:195], v[156:159]\n\t"
  "s_waitcnt lgkmcnt(2)\n\t"
  "v_mfma_f32_16x16x32_bf16 v[128:131], v[160:163], v[224:227], v[128:131]\n\t"
  "v_mfma_f32_16x16x32_bf16 v[132:135], v[160:163], v[228:231], v[132:135]\n\t"
  "ds_read_b128 v[176:179], %[aa] offset:2112\n\t"
  "v_mfma_f32_16x16x32_bf16 v[136:139], v[160:163], v[232:235], v[136:139]\n\t"
  "v_mfma_f32_16x16x32_bf16 v[140:143], v[160:163], v[236:239], v[140:143]\n\t"
  "ds_read_b128 v[180:183], %[aa] offset:2624\n\t"
  "v_mfma_f32_16x16x32_bf16 v[144:147], v[160:163], v[240:243], v[144:147]\n\t"
  "v_mfma_f32_16x16x32_bf16 v[148:151], v[160:163], v[244:247], v[148:151]\n\t"
  "ds_read_b128 v[184:187], %[aa] offset:3136\n\t"
  "v_mfma_f32_16x16x32_bf16 v[152:155], v[160:163], v[248:251], v[152:155]\n\t"
  "s_waitcnt lgkmcnt(3)\n\t"
  "v_mfma_f32_16x16x32_bf16 v[156:159], v[160:163], v[252:255], v[156:159]\n\t"
  "ds_read_b128 v[188:191], %[aa] offset:3648\n\t"
  "v_mfma_f32_16x16x32_bf16 v[132:135], v[164:167], v[224:227], v[132:135]\n\t"
  "v_mfma_f32_16x16x32_bf16 v[136:139], v[164:167], v[228:231], v[136:139]\n\t"
  "ds_read_b128 v[192:195], %[ab] offset:3712\n\t"
  "v_mfma_f32_16x16x32_bf16 v[140:143], v[164:167], v[232:235], v[140:143]\n\t"
  "v_mfma_f32_16x16x32_bf16 v[144:147], v[164:167], v[236:239], v[144:147]\n\t"
  "ds_read_b128 v[160:163], %[aa] offset:128\n\t"
  "v_mfma_f32_16x16x32_bf16 v[148:151], v[164:167], v[240:243], v[148:151]\n\t"
  "v_mfma_f32_16x16x32_bf16 v[152:155], v[164:167], v[244:247], v[152:155]\n\t"
  "ds_read_b128 v[196:199], %[ab] offset:3200\n\t"
  "v_mfma_f32_16x16x32_bf16 v[156:159], v[164:167], v[248:251], v[156:159]\n\t"
  "v_mfma_f32_16x16x32_bf16 v[136:139], v[168:171], v[224:227], v[136:139]\n\t"
  "ds_read_b128 v[200:203], %[ab] offset:2688\n\t"
  "v_mfma_f32_16x16x32_bf16 v[140:143], v[168:171], v[228:231], v[140:143]\n\t"
  "v_mfma_f32_16x16x32_bf16 v[144:147], v[168:171], v[232:235], v[144:147]\n\t"
  "ds_read_b128 v[164:167], %[aa] offset:640\n\t"
  "v_mfma_f32_16x16x32_bf16 v[148:151], v[168:171], v[236:239], v[148:151]\n\t"
  "v_mfma_f32_16x16x32_bf16 v[152:155], v[168:171], v[240:243], v[152:155]\n\t"
  "ds_read_b128 v[204:207], %[ab] offset:2176\n\t"
  "v_mfma_f32_16x16x32_bf16 v[156:159], v[168:171], v[244:247], v[156:159]\n\t"
  "v_mfma_f32_16x16x32_bf16 v[140:143], v[172:175], v[224:227], v[140:143]\n\t"
  "ds_read_b128 v[208:211], %[ab] offset:1664\n\t"
  "v_mfma_f32_16x16x32_bf16 v[144:147], v[172:175], v[228:231], v[144:147]\n\t"
  "v_mfma_f32_16x16x32_bf16 v[148:151], v[172:175], v[232:235], v[148:151]\n\t"
  "ds_read_b128 v[168:171], %[aa] offset:1152\n\t"
  "v_mfma_f32_16x16x32_bf16 v[152:155], v[172:175], v[236:239], v[152:155]\n\t"
  "v_mfma_f32_16x16x32_bf16 v[156:159], v[172:175], v[240:243], v[156:159]\n\t"
  "ds_read_b128 v[212:215], %[ab] offset:1152\n\t"
  "s_waitcnt lgkmcnt(9)\n\t"
  "v_mfma_f32_16x16x32_bf16 v[144:147], v[176:179], v[224:227], v[144:147]\n\t"
  "v_mfma_f32_16x16x32_bf16 v[148:151], v[176:179], v[228:231], v[148:151]\n\t"
  "ds_read_b128 v[216:219], %[ab] offset:640\n\t"
  "v_mfma_f32_16x16x32_bf16 v[152:155], v[176:179], v[232:235], v[152:155]\n\t"
  "v_mfma_f32_16x16x32_bf16 v[156:159], v[176:179], v[236:239], v[156:159]\n\t"
  "ds_read_b128 v[172:175], %[aa] offset:1664\n\t"
  "v_mfma_f32_16x16x32_bf16 v[148:151], v[180:183], v[224:227], v[148:151]\n\t"
  "v_mfma_f32_16x16x32_bf16 v[152:155], v[180:183], v[228:231], v[152:155]\n\t"
  "ds_read_b128 v[220:223], %[ab] offset:128\n\t"
  "v_mfma_f32_16x16x32_bf16 v[156:159], v[180:183], v[232:235], v[156:159]\n\t"
  "v_add_u32 %[aa], 0x80, %[aa]\n\t"
  "v_mfma_f32_16x16x32_bf16 v[152:155], v[184:187], v[224:227], v[152:155]\n\t"
  "v_add_u32 %[ab], 0x80, %[ab]\n\t"
  "v_mfma_f32_16x16x32_bf16 v[156:159], v[184:187], v[228:231], v[156:159]\n\t"
  "v_mfma_f32_16x16x32_bf16 v[156:159], v[188:191], v[224:227], v[156:159]\n\t"
  "s_cbranch_scc1 1b\n\t"
  "s_nop 6\n\t"
  "v_cvt_pk_bf16_f32 %[y0], v128, v129\n\t"
  "v_cvt_pk_bf16_f32 %[y1], v130, v131\n\t"
  "v_cvt_pk_bf16_f32 %[y2], v132, v133\n\t"
  "v_cvt_pk_bf16_f32 %[y3], v134, v135\n\t"
  "v_cvt_pk_bf16_f32 %[y4], v136, v137\n\t"
  "v_cvt_pk_bf16_f32 %[y5], v138, v139\n\t"
  "v_cvt_pk_bf16_f32 %[y6], v140, v141\n\t"
  "v_cvt_pk_bf16_f32 %[y7], v142, v143\n\t"
  "v_cvt_pk_bf16_f32 %[y8], v144, v145\n\t"
  "v_cvt_pk_bf16_f32 %[y9], v146, v147\n\t"
  "v_cvt_pk_bf16_f32 %[y10], v148, v149\n\t"
  "v_cvt_pk_bf16_f32 %[y11], v150, v151\n\t"
  "v_cvt_pk_bf16_f32 %[y12], v152, v153\n\t"
  "v_cvt_pk_bf16_f32 %[y13], v154, v155\n\t"
  "v_cvt_pk_bf16_f32 %[y14], v156, v157\n\t"
  "v_cvt_pk_bf16_f32 %[y15], v158, v159\n\t"
  "s_waitcnt lgkmcnt(0)\n\t"
  : [aa]"+v"(aa),[ab]"+v"(ab),[cnt]"+s"(scnt),[y0]"=&v"(y[0]),[y1]"=&v"(y[1]),[y2]"=&v"(y[2]),[y3]"=&v"(y[3]),[y4]"=&v"(y[4]),[y5]"=&v"(y[5]),[y6]"=&v"(y[6]),[y7]"=&v"(y[7]),[y8]"=&v"(y[8]),[y9]"=&v"(y[9]),[y10]"=&v"(y[10]),[y11]"=&v"(y[11]),[y12]"=&v"(y[12]),[y13]"=&v"(y[13]),[y14]"=&v"(y[14]),[y15]"=&v"(y[15])
  : [ad]"v"(AD)
  : "memory","scc","v128","v129","v130","v131","v132","v133","v134","v135","v136","v137","v138","v139","v140","v141","v142","v143","v144","v145","v146","v147","v148","v149","v150","v151","v152","v153","v154","v155","v156","v157","v158","v159","v160","v161","v162","v163","v164","v165","v166","v167","v168","v169","v170","v171","v172","v173","v174","v175","v176","v177","v178","v179","v180","v181","v182","v183","v184","v185","v186","v187","v188","v189","v190","v191","v192","v193","v194","v195","v196","v197","v198","v199","v200","v201","v202","v203","v204","v205","v206","v207","v208","v209","v210","v211","v212","v213","v214","v215","v216","v217","v218","v219","v220","v221","v222","v223","v224","v225","v226","v227","v228","v229","v230","v231","v232","v233","v234","v235","v236","v237","v238","v239","v240","v241","v242","v243","v244","v245","v246","v247","v248","v249","v250","v251","v252","v253","v254","v255");

}
DI void put_y(const unsigned (&y)[16],const char* yb,unsigned tag,int lane){
  const unsigned yo=(lane>>3)*(64*640)+(lane&7)*16;
  asm volatile("global_store_dwordx4 %0, %1, %6 sc1\n\tglobal_store_dwordx4 %0, %2, %6 offset:128 sc1\n\tglobal_store_dwordx4 %0, %3, %6 offset:256 sc1\n\t"
               "global_store_dwordx4 %0, %4, %6 offset:384 sc1\n\tglobal_store_dwordx4 %0, %5, %6 offset:512 sc1\n\ts_nop 1"
    ::"v"(yo),"v"(u32x4{tag|(y[15]&127u),y[0],y[1],y[2]}),"v"(u32x4{tag|((y[15]>>7)&127u),y[3],y[4],y[5]}),"v"(u32x4{tag|((y[15]>>14)&127u),y[6],y[7],y[8]}),
      "v"(u32x4{tag|((y[15]>>21)&127u),y[9],y[10],y[11]}),"v"(u32x4{tag|(y[15]>>28),y[12],y[13],y[14]}),"s"(yb):"memory");
}

__global__ __launch_bounds__(512,2) void fused(const u16* __restrict__ x,const u16* __restrict__ Cr,const u16* __restrict__ Ci,char* __restrict__ ws,const u16* __restrict__ Wr,const u16* __restrict__ Wi,
     u16* __restrict__ out,const u16* __restrict__ sk,const u16* __restrict__ qg,const u16* __restrict__ kg,const u16* __restrict__ vg,const u16* __restrict__ hk,const u16* __restrict__ Dv){
  extern __shared__ __attribute__((aligned(16))) char smem[];
  const int tid=threadIdx.x, lane=tid&63, wid=__builtin_amdgcn_readfirstlane(tid>>6);
  const int xcd=blockIdx.x&7, loc=blockIdx.x>>3;
  const int rot = xcd*4;
  unsigned vc[4], vx[4];
  const char *cb0=nullptr, *xb0=nullptr;
  if(loc<20 && wid>=4){
    const int L=wid-4, i=xcd*4+loc/5, j=loc%5;
    #pragma unroll
    for(int p=0;p<4;++p){
      int row=8*p+(lane>>3); int sw=((p&1)*4+(lane>>4))&7;
      vc[p]=row*4096 + (((lane&7)^sw)<<4) - 1024*p;
      int xr=4*p+(lane>>4);
      vx[p]=xr*1280 + (((lane&15)^(((lane>>4)|((p&2)<<1))<<1))<<4) - 1024*p;
    }
    unsigned co=(i*64+(L&1)*32)*4096, xo=j*256+L*16*1280;
    asm volatile("":"+v"(vc[0]),"+v"(vc[1]),"+v"(vc[2]),"+v"(vc[3]),"+v"(vx[0]),"+v"(vx[1]),"+v"(vx[2]),"+v"(vx[3]),"+s"(co),"+s"(xo));
    cb0=(const char*)((L>>1)? Ci : Cr)+co; xb0=(const char*)x+xo;
    const char* c=cb0+rot*128; const char* xx=xb0+(size_t)rot*81920;
    glds4(L*4096,vc,c); glds4(16384+L*4096,vx,xx);
  }
  asm volatile("":::"memory");
  if(loc>=20){
    if(loc>=30 && wid<CODEW){
      const char* pc; asm volatile("s_getpc_b64 %0":"=s"(pc));
      const unsigned o=((loc-30)*CODEW+wid)*8192+lane*128;
      pf((const char*)((size_t)pc&~(size_t)127)+(o<CODEB? o : 0u));
    }
    int g=(wid*12+loc-20)*64+lane+1152, sg=g/1152; g-=sg*1152; const int ks=(rot+sg)&31;
    pf(g<512? (const char*)((g&64)?Ci:Cr)+(size_t)(xcd*256+(g>>7)*64+(g&63))*4096+ks*128 : (const char*)x+(size_t)ks*81920+(g-512)*128);
  }
  asm volatile("":::"memory");
  if(loc<20){
    const int i = xcd*4 + loc/5, j=loc%5;
    auto finish=[&](){
      #pragma unroll
      for(int it=0;it<2;++it){
        int t=(tid>>4)+32*it, c=tid&15;
        const char* a=smem+t*256+((c^(t&15))<<4);
        u32x4 p=*(const u32x4*)a, q=*(const u32x4*)(a+16384), o;
        #pragma unroll
        for(int e=0;e<4;++e) o[e]=cvtpk(bflo(p[e])+bflo(q[e]),bfhi(p[e])+bfhi(q[e]));
        st_out(out+(size_t)(i*64+t)*2*D+j*128+c*8,o);
      }
    };
    if(wid>=4){
      const int L=wid-4;
      const char* cb=cb0; const char* xb=xb0;
      const char* wb = (const char*)((L>>1)? Wi : Wr) + (size_t)(j*128 + (L&1)*64)*1280;
      unsigned vw[4];
      #pragma unroll
      for(int p=0;p<4;++p){ int row=8*p+(lane>>3); int sw=((lane>>4)&1)|(p<<1); vw[p]=row*1280 + (((lane&7)^sw)<<4) - 1024*p; }
      auto issue=[&](int stg){
        int ks=(stg+rot)&31;
        unsigned slot=(stg&3)*SLOT;
        const char* c=cb+ks*128; const char* xx=xb+(size_t)ks*64*1280;
        glds4(slot+L*4096,vc,c); glds4(slot+16384+L*4096,vx,xx);
      };
      auto issuew=[&](int st){
        const char* w=wb+wcol(j,st); unsigned m=(st%5)*SLOT+L*8192;
        glds4(m,vw,w); glds4(m+4096,vw,w+32*1280);
      };
      issue(1);
      wait_vm<8>(); bar();
      issue(2);
      wait_vm<8>(); bar();
      #pragma unroll 1
      for(int s=1;s<RSN;++s){ issue(s+2); wait_vm<8>(); __builtin_amdgcn_s_sleep(RSL); bar(); }
      #pragma unroll 1
      for(int s=RSN;s<30;++s){ issue(s+2); wait_vm<8>(); bar(); }
      issuew(0); wait_vm<8>(); bar();
      issuew(1); wait_vm<0>(); bar();
      issuew(2); bar();
      issuew(3); issuew(4); issuew(5);
      wait_vm<16>(); bar();
      __builtin_amdgcn_s_sleep(W6D);
      issuew(6);
      wait_vm<8>(); bar();
      issuew(7); bar();
      issuew(8); wait_vm<16>(); bar();
      issuew(9); wait_vm<16>(); bar();
      wait_vm<8>(); bar();
      wait_vm<0>(); bar();
      bar();
      finish();
      asm volatile("s_nop 0":::"memory");
    } else {
      u64 tag;
      const char* fl=ws+WS_FA+(size_t)i*8192;
      const int wd=wid&1, wp=wid>>1;
      const int g=lane>>4, r=lane&15, q=(lane>>2)&3, p=lane&3;
      unsigned ax[4], ac[4];
      #pragma unroll
      for(int dt=0;dt<4;++dt) ax[dt]=16384 + (8*g+q)*256 + (((4*wd+dt)^(q|((g&1)<<2)))<<5) + p*8;
      ac[0]=(64*wp+32*wd+r)*128 + ((g^(r>>1))<<4); ac[1]=ac[0]^64; ac[2]=ac[0]^4096; ac[3]=ac[1]^4096;
      unsigned aw=wp*16384+(8*(r>>2)+(r&3))*128+((g^(r>>1))<<4), awo=aw^(wd<<15);
      unsigned vl=lane*16, axw=131072+wid*4096+lane*16;
      unsigned ao=wp*16384+(wd*32+r)*256+((g^r)<<4);
      unsigned neg=wp;
      const char* pqb=ws+WS_PQ+(size_t)((i*2+wp)*2)*40960;
      const char* pq0=pqb+wd*40960+(8*j+4*wd)*1024; const char* pq1=pqb+(1-wd)*40960+(8*j+4*wd)*1024;
      const char* pb[4];
      #pragma unroll
      for(int n=0;n<4;++n){ const int ch=chunk_of(j,n+1); pb[n]=pqb+wd*40960+ch*8192+4096; }
      unsigned fvo=((lane>>3)*8+j)*128+(lane&7)*16;
      int k=j+1+(lane&3); if(k>=5) k-=5;
      unsigned pvo=(j*8+k)*128;
      asm volatile("":"+v"(aw),"+v"(ao),"+v"(fvo),"+v"(pvo),"+s"(pb[0]),"+s"(pb[1]),"+s"(pb[2]),"+s"(pb[3]),"+s"(fl));
      unsigned scnt,smask; f32x16 y0,y1,y2,y3;
asm volatile(
  "s_load_dwordx2 %[ep], %[epp], 0x0\n\t"
  "v_mov_b32 v32, 0\n\t"
  "v_mov_b32 v33, 0\n\t"
  "v_mov_b32 v34, 0\n\t"
  "v_mov_b32 v35, 0\n\t"
  "v_mov_b32 v36, 0\n\t"
  "v_mov_b32 v37, 0\n\t"
  "v_mov_b32 v38, 0\n\t"
  "v_mov_b32 v39, 0\n\t"
  "v_mov_b32 v40, 0\n\t"
  "v_mov_b32 v41, 0\n\t"
  "v_mov_b32 v42, 0\n\t"
  "v_mov_b32 v43, 0\n\t"
  "v_mov_b32 v44, 0\n\t"
  "v_mov_b32 v45, 0\n\t"
  "v_mov_b32 v46, 0\n\t"
  "v_mov_b32 v47, 0\n\t"
  "v_mov_b32 v48, 0\n\t"
  "v_mov_b32 v49, 0\n\t"
  "v_mov_b32 v50, 0\n\t"
  "v_mov_b32 v51, 0\n\t"
  "v_mov_b32 v52, 0\n\t"
  "v_mov_b32 v53, 0\n\t"
  "v_mov_b32 v54, 0\n\t"
  "v_mov_b32 v55, 0\n\t"
  "v_mov_b32 v56, 0\n\t"
  "v_mov_b32 v57, 0\n\t"
  "v_mov_b32 v58, 0\n\t"
  "v_mov_b32 v59, 0\n\t"
  "v_mov_b32 v60, 0\n\t"
  "v_mov_b32 v61, 0\n\t"
  "v_mov_b32 v62, 0\n\t"
  "v_mov_b32 v63, 0\n\t"
  "v_mov_b32 v64, 0\n\t"
  "v_mov_b32 v65, 0\n\t"
  "v_mov_b32 v66, 0\n\t"
  "v_mov_b32 v67, 0\n\t"
  "v_mov_b32 v68, 0\n\t"
  "v_mov_b32 v69, 0\n\t"
  "v_mov_b32 v70, 0\n\t"
  "v_mov_b32 v71, 0\n\t"
  "v_mov_b32 v72, 0\n\t"
  "v_mov_b32 v73, 0\n\t"
  "v_mov_b32 v74, 0\n\t"
  "v_mov_b32 v75, 0\n\t"
  "v_mov_b32 v76, 0\n\t"
  "v_mov_b32 v77, 0\n\t"
  "v_mov_b32 v78, 0\n\t"
  "v_mov_b32 v79, 0\n\t"
  "v_mov_b32 v80, 0\n\t"
  "v_mov_b32 v81, 0\n\t"
  "v_mov_b32 v82, 0\n\t"
  "v_mov_b32 v83, 0\n\t"
  "v_mov_b32 v84, 0\n\t"
  "v_mov_b32 v85, 0\n\t"
  "v_mov_b32 v86, 0\n\t"
  "v_mov_b32 v87, 0\n\t"
  "v_mov_b32 v88, 0\n\t"
  "v_mov_b32 v89, 0\n\t"
  "v_mov_b32 v90, 0\n\t"
  "v_mov_b32 v91, 0\n\t"
  "v_mov_b32 v92, 0\n\t"
  "v_mov_b32 v93, 0\n\t"
  "v_mov_b32 v94, 0\n\t"
  "v_mov_b32 v95, 0\n\t"
  "s_mov_b32 %[cnt], 31\n\t"
  "s_mov_b32 %[msk], 0x18000\n\t"
  "s_barrier\n\t"
  "ds_read_b128 v[112:115], %[ac0] offset:0\n\t"
  "ds_read_b64_tr_b16 v[96:97], %[ax0] offset:0\n\t"
  "ds_read_b64_tr_b16 v[98:99], %[ax0] offset:1024\n\t"
  "ds_read_b128 v[116:119], %[ac0] offset:2048\n\t"
  "ds_read_b128 v[120:123], %[ac2] offset:0\n\t"
  "ds_read_b128 v[124:127], %[ac2] offset:2048\n\t"
  "ds_read_b64_tr_b16 v[100:101], %[ax1] offset:0\n\t"
  "ds_read_b64_tr_b16 v[102:103], %[ax1] offset:1024\n\t"
  ".p2align 6\n\t"
  "1:\n\t"
  "s_waitcnt lgkmcnt(2)\n\t"
  "v_mfma_f32_16x16x32_bf16 v[32:35], v[96:99], v[112:115], v[32:35]\n\t"
  "ds_read_b64_tr_b16 v[104:105], %[ax2] offset:0\n\t"
  "v_mfma_f32_16x16x32_bf16 v[36:39], v[96:99], v[116:119], v[36:39]\n\t"
  "ds_read_b64_tr_b16 v[106:107], %[ax2] offset:1024\n\t"
  "v_mfma_f32_16x16x32_bf16 v[40:43], v[96:99], v[120:123], v[40:43]\n\t"
  "ds_read_b64_tr_b16 v[108:109], %[ax3] offset:0\n\t"
  "v_mfma_f32_16x16x32_bf16 v[44:47], v[96:99], v[124:127], v[44:47]\n\t"
  "ds_read_b64_tr_b16 v[110:111], %[ax3] offset:1024\n\t"
  "s_waitcnt lgkmcnt(4)\n\t"
  "v_mfma_f32_16x16x32_bf16 v[60:63], v[100:103], v[124:127], v[60:63]\n\t"
  "s_xor_b32 %[msk], %[msk], 0x10000\n\t"
  "v_mfma_f32_16x16x32_bf16 v[56:59], v[100:103], v[120:123], v[56:59]\n\t"
  "ds_read_b128 v[144:147], %[ac1] offset:0\n\t"
  "v_mfma_f32_16x16x32_bf16 v[52:55], v[100:103], v[116:119], v[52:55]\n\t"
  "ds_read_b64_tr_b16 v[128:129], %[ax0] offset:8192\n\t"
  "v_mfma_f32_16x16x32_bf16 v[48:51], v[100:103], v[112:115], v[48:51]\n\t"
  "ds_read_b64_tr_b16 v[130:131], %[ax0] offset:9216\n\t"
  "s_waitcnt lgkmcnt(3)\n\t"
  "v_mfma_f32_16x16x32_bf16 v[64:67], v[104:107], v[112:115], v[64:67]\n\t"
  "v_xor_b32 %[ac0], %[msk], %[ac0]\n\t"
  "v_mfma_f32_16x16x32_bf16 v[68:71], v[104:107], v[116:119], v[68:71]\n\t"
  "ds_read_b128 v[148:151], %[ac1] offset:2048\n\t"
  "v_mfma_f32_16x16x32_bf16 v[72:75], v[104:107], v[120:123], v[72:75]\n\t"
  "ds_read_b128 v[152:155], %[ac3] offset:0\n\t"
  "v_mfma_f32_16x16x32_bf16 v[76:79], v[104:107], v[124:127], v[76:79]\n\t"
  "ds_read_b128 v[156:159], %[ac3] offset:2048\n\t"
  "v_mfma_f32_16x16x32_bf16 v[92:95], v[108:111], v[124:127], v[92:95]\n\t"
  "v_xor_b32 %[ac2], %[msk], %[ac2]\n\t"
  "v_mfma_f32_16x16x32_bf16 v[88:91], v[108:111], v[120:123], v[88:91]\n\t"
  "ds_read_b64_tr_b16 v[132:133], %[ax1] offset:8192\n\t"
  "v_mfma_f32_16x16x32_bf16 v[84:87], v[108:111], v[116:119], v[84:87]\n\t"
  "ds_read_b64_tr_b16 v[134:135], %[ax1] offset:9216\n\t"
  "v_mfma_f32_16x16x32_bf16 v[80:83], v[108:111], v[112:115], v[80:83]\n\t"
  "v_xor_b32 %[ax0], %[msk], %[ax0]\n\t"
  "s_waitcnt lgkmcnt(2)\n\t"
  "v_mfma_f32_16x16x32_bf16 v[32:35], v[128:131], v[144:147], v[32:35]\n\t"
  "ds_read_b64_tr_b16 v[136:137], %[ax2] offset:8192\n\t"
  "v_mfma_f32_16x16x32_bf16 v[36:39], v[128:131], v[148:151], v[36:39]\n\t"
  "ds_read_b64_tr_b16 v[138:139], %[ax2] offset:9216\n\t"
  "s_sub_u32 %[cnt], %[cnt], 1\n\t"
  "v_mfma_f32_16x16x32_bf16 v[40:43], v[128:131], v[152:155], v[40:43]\n\t"
  "ds_read_b64_tr_b16 v[140:141], %[ax3] offset:8192\n\t"
  "v_mfma_f32_16x16x32_bf16 v[44:47], v[128:131], v[156:159], v[44:47]\n\t"
  "ds_read_b64_tr_b16 v[142:143], %[ax3] offset:9216\n\t"
  "s_cmp_lg_u32 %[cnt], 0\n\t"
  "s_waitcnt lgkmcnt(4)\n\t"
  "v_mfma_f32_16x16x32_bf16 v[60:63], v[132:135], v[156:159], v[60:63]\n\t"
  "s_barrier\n\t"
  "v_mfma_f32_16x16x32_bf16 v[56:59], v[132:135], v[152:155], v[56:59]\n\t"
  "ds_read_b128 v[112:115], %[ac0] offset:0\n\t"
  "v_mfma_f32_16x16x32_bf16 v[52:55], v[132:135], v[148:151], v[52:55]\n\t"
  "ds_read_b64_tr_b16 v[96:97], %[ax0] offset:0\n\t"
  "v_mfma_f32_16x16x32_bf16 v[48:51], v[132:135], v[144:147], v[48:51]\n\t"
  "ds_read_b64_tr_b16 v[98:99], %[ax0] offset:1024\n\t"
  "s_waitcnt lgkmcnt(3)\n\t"
  "v_mfma_f32_16x16x32_bf16 v[64:67], v[136:139], v[144:147], v[64:67]\n\t"
  "v_xor_b32 %[ax1], %[msk], %[ax1]\n\t"
  "v_xor_b32 %[ax2], %[msk], %[ax2]\n\t"
  "v_mfma_f32_16x16x32_bf16 v[68:71], v[136:139], v[148:151], v[68:71]\n\t"
  "ds_read_b128 v[116:119], %[ac0] offset:2048\n\t"
  "v_mfma_f32_16x16x32_bf16 v[72:75], v[136:139], v[152:155], v[72:75]\n\t"
  "ds_read_b128 v[120:123], %[ac2] offset:0\n\t"
  "v_mfma_f32_16x16x32_bf16 v[76:79], v[136:139], v[156:159], v[76:79]\n\t"
  "ds_read_b128 v[124:127], %[ac2] offset:2048\n\t"
  "v_mfma_f32_16x16x32_bf16 v[92:95], v[140:143], v[156:159], v[92:95]\n\t"
  "v_xor_b32 %[ax3], %[msk], %[ax3]\n\t"
  "v_xor_b32 %[ac1], %[msk], %[ac1]\n\t"
  "v_mfma_f32_16x16x32_bf16 v[88:91], v[140:143], v[152:155], v[88:91]\n\t"
  "ds_read_b64_tr_b16 v[100:101], %[ax1] offset:0\n\t"
  "v_mfma_f32_16x16x32_bf16 v[84:87], v[140:143], v[148:151], v[84:87]\n\t"
  "ds_read_b64_tr_b16 v[102:103], %[ax1] offset:1024\n\t"
  "v_mfma_f32_16x16x32_bf16 v[80:83], v[140:143], v[144:147], v[80:83]\n\t"
  "v_xor_b32 %[ac3], %[msk], %[ac3]\n\t"
  "s_cbranch_scc1 1b\n\t"
  "s_waitcnt lgkmcnt(2)\n\t"
  "v_mfma_f32_16x16x32_bf16 v[32:35], v[96:99], v[112:115], v[32:35]\n\t"
  "ds_read_b64_tr_b16 v[104:105], %[ax2] offset:0\n\t"
  "v_mfma_f32_16x16x32_bf16 v[36:39], v[96:99], v[116:119], v[36:39]\n\t"
  "ds_read_b64_tr_b16 v[106:107], %[ax2] offset:1024\n\t"
  "v_mfma_f32_16x16x32_bf16 v[40:43], v[96:99], v[120:123], v[40:43]\n\t"
  "ds_read_b64_tr_b16 v[108:109], %[ax3] offset:0\n\t"
  "v_mfma_f32_16x16x32_bf16 v[44:47], v[96:99], v[124:127], v[44:47]\n\t"
  "ds_read_b64_tr_b16 v[110:111], %[ax3] offset:1024\n\t"
  "s_waitcnt lgkmcnt(4)\n\t"
  "v_mfma_f32_16x16x32_bf16 v[60:63], v[100:103], v[124:127], v[60:63]\n\t"
  "v_mfma_f32_16x16x32_bf16 v[56:59], v[100:103], v[120:123], v[56:59]\n\t"
  "ds_read_b128 v[144:147], %[ac1] offset:0\n\t"
  "v_mfma_f32_16x16x32_bf16 v[52:55], v[100:103], v[116:119], v[52:55]\n\t"
  "ds_read_b64_tr_b16 v[128:129], %[ax0] offset:8192\n\t"
  "v_mfma_f32_16x16x32_bf16 v[48:51], v[100:103], v[112:115], v[48:51]\n\t"
  "ds_read_b64_tr_b16 v[130:131], %[ax0] offset:9216\n\t"
  "s_waitcnt lgkmcnt(3)\n\t"
  "v_mfma_f32_16x16x32_bf16 v[64:67], v[104:107], v[112:115], v[64:67]\n\t"
  "v_mfma_f32_16x16x32_bf16 v[68:71], v[104:107], v[116:119], v[68:71]\n\t"
  "ds_read_b128 v[148:151], %[ac1] offset:2048\n\t"
  "v_mfma_f32_16x16x32_bf16 v[72:75], v[104:107], v[120:123], v[72:75]\n\t"
  "ds_read_b128 v[152:155], %[ac3] offset:0\n\t"
  "v_mfma_f32_16x16x32_bf16 v[76:79], v[104:107], v[124:127], v[76:79]\n\t"
  "ds_read_b128 v[156:159], %[ac3] offset:2048\n\t"
  "v_mfma_f32_16x16x32_bf16 v[92:95], v[108:111], v[124:127], v[92:95]\n\t"
  "v_mfma_f32_16x16x32_bf16 v[88:91], v[108:111], v[120:123], v[88:91]\n\t"
  "ds_read_b64_tr_b16 v[132:133], %[ax1] offset:8192\n\t"
  "v_mfma_f32_16x16x32_bf16 v[84:87], v[108:111], v[116:119], v[84:87]\n\t"
  "ds_read_b64_tr_b16 v[134:135], %[ax1] offset:9216\n\t"
  "v_mfma_f32_16x16x32_bf16 v[80:83], v[108:111], v[112:115], v[80:83]\n\t"
  "s_waitcnt lgkmcnt(2)\n\t"
  "v_mfma_f32_16x16x32_bf16 v[32:35], v[128:131], v[144:147], v[32:35]\n\t"
  "ds_read_b64_tr_b16 v[136:137], %[ax2] offset:8192\n\t"
  "v_mfma_f32_16x16x32_bf16 v[36:39], v[128:131], v[148:151], v[36:39]\n\t"
  "ds_read_b64_tr_b16 v[138:139], %[ax2] offset:9216\n\t"
  "v_mfma_f32_16x16x32_bf16 v[40:43], v[128:131], v[152:155], v[40:43]\n\t"
  "ds_read_b64_tr_b16 v[140:141], %[ax3] offset:8192\n\t"
  "v_mfma_f32_16x16x32_bf16 v[44:47], v[128:131], v[156:159], v[44:47]\n\t"
  "ds_read_b64_tr_b16 v[142:143], %[ax3] offset:9216\n\t"
  "s_waitcnt lgkmcnt(4)\n\t"
  "v_mfma_f32_16x16x32_bf16 v[60:63], v[132:135], v[156:159], v[60:63]\n\t"
  "v_mfma_f32_16x16x32_bf16 v[56:59], v[132:135], v[152:155], v[56:59]\n\t"
  "v_mfma_f32_16x16x32_bf16 v[52:55], v[132:135], v[148:151], v[52:55]\n\t"
  "v_mfma_f32_16x16x32_bf16 v[48:51], v[132:135], v[144:147], v[48:51]\n\t"
  "s_waitcnt lgkmcnt(0)\n\t"
  "v_mfma_f32_16x16x32_bf16 v[64:67], v[136:139], v[144:147], v[64:67]\n\t"
  "v_mfma_f32_16x16x32_bf16 v[68:71], v[136:139], v[148:151], v[68:71]\n\t"
  "v_mfma_f32_16x16x32_bf16 v[72:75], v[136:139], v[152:155], v[72:75]\n\t"
  "v_mfma_f32_16x16x32_bf16 v[76:79], v[136:139], v[156:159], v[76:79]\n\t"
  "v_mfma_f32_16x16x32_bf16 v[92:95], v[140:143], v[156:159], v[92:95]\n\t"
  "v_mfma_f32_16x16x32_bf16 v[88:91], v[140:143], v[152:155], v[88:91]\n\t"
  "v_mfma_f32_16x16x32_bf16 v[84:87], v[140:143], v[148:151], v[84:87]\n\t"
  "v_mfma_f32_16x16x32_bf16 v[80:83], v[140:143], v[144:147], v[80:83]\n\t"
  "s_cmp_lg_u32 %[neg], 0\n\t"
  "s_cbranch_scc1 2f\n\t"
  "v_cvt_pk_bf16_f32 v160, v32, v33\n\t"
  "v_cvt_pk_bf16_f32 v162, v48, v49\n\t"
  "v_cvt_pk_bf16_f32 v161, v34, v35\n\t"
  "v_cvt_pk_bf16_f32 v163, v50, v51\n\t"
  "v_cvt_pk_bf16_f32 v164, v36, v37\n\t"
  "v_cvt_pk_bf16_f32 v166, v52, v53\n\t"
  "v_cvt_pk_bf16_f32 v165, v38, v39\n\t"
  "v_cvt_pk_bf16_f32 v167, v54, v55\n\t"
  "v_permlane32_swap_b32 v160, v162\n\t"
  "v_permlane32_swap_b32 v161, v163\n\t"
  "v_cvt_pk_bf16_f32 v240, v40, v41\n\t"
  "v_cvt_pk_bf16_f32 v242, v56, v57\n\t"
  "v_cvt_pk_bf16_f32 v241, v42, v43\n\t"
  "v_cvt_pk_bf16_f32 v243, v58, v59\n\t"
  "v_permlane32_swap_b32 v164, v166\n\t"
  "v_permlane32_swap_b32 v165, v167\n\t"
  "v_permlane16_swap_b32 v160, v162\n\t"
  "v_permlane16_swap_b32 v161, v163\n\t"
  "global_store_dwordx4 %[vl], v[160:163], %[pq0] offset:0 sc1\n\t"
  "v_cvt_pk_bf16_f32 v244, v44, v45\n\t"
  "v_cvt_pk_bf16_f32 v246, v60, v61\n\t"
  "v_cvt_pk_bf16_f32 v245, v46, v47\n\t"
  "v_cvt_pk_bf16_f32 v247, v62, v63\n\t"
  "v_permlane32_swap_b32 v240, v242\n\t"
  "v_permlane32_swap_b32 v241, v243\n\t"
  "v_permlane16_swap_b32 v164, v166\n\t"
  "v_permlane16_swap_b32 v165, v167\n\t"
  "global_store_dwordx4 %[vl], v[164:167], %[pq0] offset:1024 sc1\n\t"
  "v_cvt_pk_bf16_f32 v252, v76, v77\n\t"
  "v_cvt_pk_bf16_f32 v254, v92, v93\n\t"
  "v_cvt_pk_bf16_f32 v253, v78, v79\n\t"
  "v_cvt_pk_bf16_f32 v255, v94, v95\n\t"
  "v_permlane32_swap_b32 v244, v246\n\t"
  "v_permlane32_swap_b32 v245, v247\n\t"
  "v_permlane16_swap_b32 v240, v242\n\t"
  "v_permlane16_swap_b32 v241, v243\n\t"
  "global_store_dwordx4 %[vl], v[240:243], %[pq1] offset:0 sc1\n\t"
  "v_cvt_pk_bf16_f32 v248, v72, v73\n\t"
  "v_cvt_pk_bf16_f32 v250, v88, v89\n\t"
  "v_cvt_pk_bf16_f32 v249, v74, v75\n\t"
  "v_cvt_pk_bf16_f32 v251, v90, v91\n\t"
  "v_permlane32_swap_b32 v252, v254\n\t"
  "v_permlane32_swap_b32 v253, v255\n\t"
  "v_permlane16_swap_b32 v244, v246\n\t"
  "v_permlane16_swap_b32 v245, v247\n\t"
  "global_store_dwordx4 %[vl], v[244:247], %[pq1] offset:1024 sc1\n\t"
  "v_cvt_pk_bf16_f32 v172, v68, v69\n\t"
  "v_cvt_pk_bf16_f32 v174, v84, v85\n\t"
  "v_cvt_pk_bf16_f32 v173, v70, v71\n\t"
  "v_cvt_pk_bf16_f32 v175, v86, v87\n\t"
  "v_permlane32_swap_b32 v248, v250\n\t"
  "v_permlane32_swap_b32 v249, v251\n\t"
  "v_permlane16_swap_b32 v252, v254\n\t"
  "v_permlane16_swap_b32 v253, v255\n\t"
  "global_store_dwordx4 %[vl], v[252:255], %[pq1] offset:3072 sc1\n\t"
  "v_cvt_pk_bf16_f32 v168, v64, v65\n\t"
  "v_cvt_pk_bf16_f32 v170, v80, v81\n\t"
  "v_cvt_pk_bf16_f32 v169, v66, v67\n\t"
  "v_cvt_pk_bf16_f32 v171, v82, v83\n\t"
  "v_permlane32_swap_b32 v172, v174\n\t"
  "v_permlane32_swap_b32 v173, v175\n\t"
  "v_permlane16_swap_b32 v248, v250\n\t"
  "v_permlane16_swap_b32 v249, v251\n\t"
  "global_store_dwordx4 %[vl], v[248:251], %[pq1] offset:2048 sc1\n\t"
  "v_permlane32_swap_b32 v168, v170\n\t"
  "v_permlane32_swap_b32 v169, v171\n\t"
  "v_permlane16_swap_b32 v172, v174\n\t"
  "v_permlane16_swap_b32 v173, v175\n\t"
  "global_store_dwordx4 %[vl], v[172:175], %[pq0] offset:3072 sc1\n\t"
  "s_nop 0\n\t"
  "v_permlane16_swap_b32 v168, v170\n\t"
  "v_permlane16_swap_b32 v169, v171\n\t"
  "global_store_dwordx4 %[vl], v[168:171], %[pq0] offset:2048 sc1\n\t"
  "s_branch 3f\n\t"
  "2:\n\t"
  "v_cvt_pk_bf16_f32 v160, -v32, -v33\n\t"
  "v_cvt_pk_bf16_f32 v162, -v48, -v49\n\t"
  "v_cvt_pk_bf16_f32 v161, -v34, -v35\n\t"
  "v_cvt_pk_bf16_f32 v163, -v50, -v51\n\t"
  "v_cvt_pk_bf16_f32 v164, -v36, -v37\n\t"
  "v_cvt_pk_bf16_f32 v166, -v52, -v53\n\t"
  "v_cvt_pk_bf16_f32 v165, -v38, -v39\n\t"
  "v_cvt_pk_bf16_f32 v167, -v54, -v55\n\t"
  "v_permlane32_swap_b32 v160, v162\n\t"
  "v_permlane32_swap_b32 v161, v163\n\t"
  "v_cvt_pk_bf16_f32 v240, -v40, -v41\n\t"
  "v_cvt_pk_bf16_f32 v242, -v56, -v57\n\t"
  "v_cvt_pk_bf16_f32 v241, -v42, -v43\n\t"
  "v_cvt_pk_bf16_f32 v243, -v58, -v59\n\t"
  "v_permlane32_swap_b32 v164, v166\n\t"
  "v_permlane32_swap_b32 v165, v167\n\t"
  "v_permlane16_swap_b32 v160, v162\n\t"
  "v_permlane16_swap_b32 v161, v163\n\t"
  "global_store_dwordx4 %[vl], v[160:163], %[pq0] offset:0 sc1\n\t"
  "v_cvt_pk_bf16_f32 v244, -v44, -v45\n\t"
  "v_cvt_pk_bf16_f32 v246, -v60, -v61\n\t"
  "v_cvt_pk_bf16_f32 v245, -v46, -v47\n\t"
  "v_cvt_pk_bf16_f32 v247, -v62, -v63\n\t"
  "v_permlane32_swap_b32 v240, v242\n\t"
  "v_permlane32_swap_b32 v241, v243\n\t"
  "v_permlane16_swap_b32 v164, v166\n\t"
  "v_permlane16_swap_b32 v165, v167\n\t"
  "global_store_dwordx4 %[vl], v[164:167], %[pq0] offset:1024 sc1\n\t"
  "v_cvt_pk_bf16_f32 v252, -v76, -v77\n\t"
  "v_cvt_pk_bf16_f32 v254, -v92, -v93\n\t"
  "v_cvt_pk_bf16_f32 v253, -v78, -v79\n\t"
  "v_cvt_pk_bf16_f32 v255, -v94, -v95\n\t"
  "v_permlane32_swap_b32 v244, v246\n\t"
  "v_permlane32_swap_b32 v245, v247\n\t"
  "v_permlane16_swap_b32 v240, v242\n\t"
  "v_permlane16_swap_b32 v241, v243\n\t"
  "global_store_dwordx4 %[vl], v[240:243], %[pq1] offset:0 sc1\n\t"
  "v_cvt_pk_bf16_f32 v248, -v72, -v73\n\t"
  "v_cvt_pk_bf16_f32 v250, -v88, -v89\n\t"
  "v_cvt_pk_bf16_f32 v249, -v74, -v75\n\t"
  "v_cvt_pk_bf16_f32 v251, -v90, -v91\n\t"
  "v_permlane32_swap_b32 v252, v254\n\t"
  "v_permlane32_swap_b32 v253, v255\n\t"
  "v_permlane16_swap_b32 v244, v246\n\t"
  "v_permlane16_swap_b32 v245, v247\n\t"
  "global_store_dwordx4 %[vl], v[244:247], %[pq1] offset:1024 sc1\n\t"
  "v_cvt_pk_bf16_f32 v172, -v68, -v69\n\t"
  "v_cvt_pk_bf16_f32 v174, -v84, -v85\n\t"
  "v_cvt_pk_bf16_f32 v173, -v70, -v71\n\t"
  "v_cvt_pk_bf16_f32 v175, -v86, -v87\n\t"
  "v_permlane32_swap_b32 v248, v250\n\t"
  "v_permlane32_swap_b32 v249, v251\n\t"
  "v_permlane16_swap_b32 v252, v254\n\t"
  "v_permlane16_swap_b32 v253, v255\n\t"
  "global_store_dwordx4 %[vl], v[252:255], %[pq1] offset:3072 sc1\n\t"
  "v_cvt_pk_bf16_f32 v168, -v64, -v65\n\t"
  "v_cvt_pk_bf16_f32 v170, -v80, -v81\n\t"
  "v_cvt_pk_bf16_f32 v169, -v66, -v67\n\t"
  "v_cvt_pk_bf16_f32 v171, -v82, -v83\n\t"
  "v_permlane32_swap_b32 v172, v174\n\t"
  "v_permlane32_swap_b32 v173, v175\n\t"
  "v_permlane16_swap_b32 v248, v250\n\t"
  "v_permlane16_swap_b32 v249, v251\n\t"
  "global_store_dwordx4 %[vl], v[248:251], %[pq1] offset:2048 sc1\n\t"
  "v_permlane32_swap_b32 v168, v170\n\t"
  "v_permlane32_swap_b32 v169, v171\n\t"
  "v_permlane16_swap_b32 v172, v174\n\t"
  "v_permlane16_swap_b32 v173, v175\n\t"
  "global_store_dwordx4 %[vl], v[172:175], %[pq0] offset:3072 sc1\n\t"
  "s_nop 0\n\t"
  "v_permlane16_swap_b32 v168, v170\n\t"
  "v_permlane16_swap_b32 v169, v171\n\t"
  "global_store_dwordx4 %[vl], v[168:171], %[pq0] offset:2048 sc1\n\t"
  "3:\n\t"
  "ds_write_b128 %[axw], v[240:243] offset:0\n\t"
  "ds_write_b128 %[axw], v[244:247] offset:1024\n\t"
  "ds_write_b128 %[axw], v[252:255] offset:3072\n\t"
  "ds_write_b128 %[axw], v[248:251] offset:2048\n\t"
  "s_waitcnt lgkmcnt(0)\n\t"
  "s_barrier\n\t"
  "v_xor_b32 %[ac0], 0x0, %[awo]\n\t"
  "ds_read_b128 v[96:99], %[ac0] offset:0\n\t"
  "ds_read_b128 v[100:103], %[ac0] offset:512\n\t"
  "ds_read_b128 v[104:107], %[ac0] offset:4096\n\t"
  "ds_read_b128 v[108:111], %[ac0] offset:4608\n\t"
  "ds_read_b128 v[112:115], %[ac0] offset:8192\n\t"
  "v_xor_b32 %[ac2], 0x1000, %[axw]\n\t"
  "ds_read_b128 v[176:179], %[ac2] offset:0\n\t"
  "ds_read_b128 v[180:183], %[ac2] offset:1024\n\t"
  "ds_read_b128 v[184:187], %[ac2] offset:2048\n\t"
  "ds_read_b128 v[188:191], %[ac2] offset:3072\n\t"
  "s_waitcnt lgkmcnt(7)\n\t"
  "v_mfma_f32_16x16x32_bf16 v[32:35], v[96:99], v[160:163], 0\n\t"
  "ds_read_b128 v[116:119], %[ac0] offset:8704\n\t"
  "v_mfma_f32_16x16x32_bf16 v[36:39], v[96:99], v[164:167], 0\n\t"
  "v_mfma_f32_16x16x32_bf16 v[40:43], v[100:103], v[160:163], 0\n\t"
  "ds_read_b128 v[120:123], %[ac0] offset:12288\n\t"
  "v_mfma_f32_16x16x32_bf16 v[44:47], v[100:103], v[164:167], 0\n\t"
  "s_waitcnt lgkmcnt(7)\n\t"
  "v_mfma_f32_16x16x32_bf16 v[48:51], v[104:107], v[160:163], 0\n\t"
  "ds_read_b128 v[124:127], %[ac0] offset:12800\n\t"
  "v_mfma_f32_16x16x32_bf16 v[52:55], v[104:107], v[164:167], 0\n\t"
  "v_mfma_f32_16x16x32_bf16 v[56:59], v[108:111], v[160:163], 0\n\t"
  "v_xor_b32 %[ac1], 0x40, %[awo]\n\t"
  "ds_read_b128 v[96:99], %[ac1] offset:0\n\t"
  "v_mfma_f32_16x16x32_bf16 v[60:63], v[108:111], v[164:167], 0\n\t"
  "s_waitcnt lgkmcnt(3)\n\t"
  "v_mfma_f32_16x16x32_bf16 v[64:67], v[112:115], v[160:163], 0\n\t"
  "ds_read_b128 v[100:103], %[ac1] offset:512\n\t"
  "v_mfma_f32_16x16x32_bf16 v[68:71], v[112:115], v[164:167], 0\n\t"
  "v_mfma_f32_16x16x32_bf16 v[72:75], v[116:119], v[160:163], 0\n\t"
  "ds_read_b128 v[104:107], %[ac1] offset:4096\n\t"
  "v_mfma_f32_16x16x32_bf16 v[76:79], v[116:119], v[164:167], 0\n\t"
  "s_waitcnt lgkmcnt(3)\n\t"
  "v_mfma_f32_16x16x32_bf16 v[80:83], v[120:123], v[160:163], 0\n\t"
  "ds_read_b128 v[108:111], %[ac1] offset:4608\n\t"
  "v_mfma_f32_16x16x32_bf16 v[84:87], v[120:123], v[164:167], 0\n\t"
  "v_mfma_f32_16x16x32_bf16 v[88:91], v[124:127], v[160:163], 0\n\t"
  "ds_read_b128 v[112:115], %[ac1] offset:8192\n\t"
  "v_mfma_f32_16x16x32_bf16 v[92:95], v[124:127], v[164:167], 0\n\t"
  "s_waitcnt lgkmcnt(3)\n\t"
  "v_mfma_f32_16x16x32_bf16 v[32:35], v[96:99], v[168:171], v[32:35]\n\t"
  "ds_read_b128 v[116:119], %[ac1] offset:8704\n\t"
  "v_mfma_f32_16x16x32_bf16 v[36:39], v[96:99], v[172:175], v[36:39]\n\t"
  "v_mfma_f32_16x16x32_bf16 v[40:43], v[100:103], v[168:171], v[40:43]\n\t"
  "ds_read_b128 v[120:123], %[ac1] offset:12288\n\t"
  "v_mfma_f32_16x16x32_bf16 v[44:47], v[100:103], v[172:175], v[44:47]\n\t"
  "s_waitcnt lgkmcnt(3)\n\t"
  "v_mfma_f32_16x16x32_bf16 v[48:51], v[104:107], v[168:171], v[48:51]\n\t"
  "ds_read_b128 v[124:127], %[ac1] offset:12800\n\t"
  "v_mfma_f32_16x16x32_bf16 v[52:55], v[104:107], v[172:175], v[52:55]\n\t"
  "v_mfma_f32_16x16x32_bf16 v[56:59], v[108:111], v[168:171], v[56:59]\n\t"
  "v_xor_b32 %[ac0], 0x8000, %[awo]\n\t"
  "ds_read_b128 v[96:99], %[ac0] offset:0\n\t"
  "v_mfma_f32_16x16x32_bf16 v[60:63], v[108:111], v[172:175], v[60:63]\n\t"
  "s_waitcnt lgkmcnt(3)\n\t"
  "v_mfma_f32_16x16x32_bf16 v[64:67], v[112:115], v[168:171], v[64:67]\n\t"
  "ds_read_b128 v[100:103], %[ac0] offset:512\n\t"
  "v_mfma_f32_16x16x32_bf16 v[68:71], v[112:115], v[172:175], v[68:71]\n\t"
  "v_mfma_f32_16x16x32_bf16 v[72:75], v[116:119], v[168:171], v[72:75]\n\t"
  "ds_read_b128 v[104:107], %[ac0] offset:4096\n\t"
  "v_mfma_f32_16x16x32_bf16 v[76:79], v[116:119], v[172:175], v[76:79]\n\t"
  "s_waitcnt lgkmcnt(3)\n\t"
  "v_mfma_f32_16x16x32_bf16 v[80:83], v[120:123], v[168:171], v[80:83]\n\t"
  "ds_read_b128 v[108:111], %[ac0] offset:4608\n\t"
  "v_mfma_f32_16x16x32_bf16 v[84:87], v[120:123], v[172:175], v[84:87]\n\t"
  "v_mfma_f32_16x16x32_bf16 v[88:91], v[124:127], v[168:171], v[88:91]\n\t"
  "ds_read_b128 v[112:115], %[ac0] offset:8192\n\t"
  "v_mfma_f32_16x16x32_bf16 v[92:95], v[124:127], v[172:175], v[92:95]\n\t"
  "s_waitcnt lgkmcnt(3)\n\t"
  "v_mfma_f32_16x16x32_bf16 v[32:35], v[96:99], v[176:179], v[32:35]\n\t"
  "ds_read_b128 v[116:119], %[ac0] offset:8704\n\t"
  "v_mfma_f32_16x16x32_bf16 v[36:39], v[96:99], v[180:183], v[36:39]\n\t"
  "v_mfma_f32_16x16x32_bf16 v[40:43], v[100:103], v[176:179], v[40:43]\n\t"
  "ds_read_b128 v[120:123], %[ac0] offset:12288\n\t"
  "v_mfma_f32_16x16x32_bf16 v[44:47], v[100:103], v[180:183], v[44:47]\n\t"
  "s_waitcnt lgkmcnt(3)\n\t"
  "v_mfma_f32_16x16x32_bf16 v[48:51], v[104:107], v[176:179], v[48:51]\n\t"
  "ds_read_b128 v[124:127], %[ac0] offset:12800\n\t"
  "v_mfma_f32_16x16x32_bf16 v[52:55], v[104:107], v[180:183], v[52:55]\n\t"
  "v_mfma_f32_16x16x32_bf16 v[56:59], v[108:111], v[176:179], v[56:59]\n\t"
  "v_xor_b32 %[ac1], 0x8040, %[awo]\n\t"
  "ds_read_b128 v[96:99], %[ac1] offset:0\n\t"
  "v_mfma_f32_16x16x32_bf16 v[60:63], v[108:111], v[180:183], v[60:63]\n\t"
  "s_waitcnt lgkmcnt(3)\n\t"
  "v_mfma_f32_16x16x32_bf16 v[64:67], v[112:115], v[176:179], v[64:67]\n\t"
  "ds_read_b128 v[100:103], %[ac1] offset:512\n\t"
  "v_mfma_f32_16x16x32_bf16 v[68:71], v[112:115], v[180:183], v[68:71]\n\t"
  "v_mfma_f32_16x16x32_bf16 v[72:75], v[116:119], v[176:179], v[72:75]\n\t"
  "ds_read_b128 v[104:107], %[ac1] offset:4096\n\t"
  "v_mfma_f32_16x16x32_bf16 v[76:79], v[116:119], v[180:183], v[76:79]\n\t"
  "s_waitcnt lgkmcnt(3)\n\t"
  "v_mfma_f32_16x16x32_bf16 v[80:83], v[120:123], v[176:179], v[80:83]\n\t"
  "ds_read_b128 v[108:111], %[ac1] offset:4608\n\t"
  "v_mfma_f32_16x16x32_bf16 v[84:87], v[120:123], v[180:183], v[84:87]\n\t"
  "v_mfma_f32_16x16x32_bf16 v[88:91], v[124:127], v[176:179], v[88:91]\n\t"
  "ds_read_b128 v[112:115], %[ac1] offset:8192\n\t"
  "v_mfma_f32_16x16x32_bf16 v[92:95], v[124:127], v[180:183], v[92:95]\n\t"
  "s_waitcnt lgkmcnt(3)\n\t"
  "v_mfma_f32_16x16x32_bf16 v[32:35], v[96:99], v[184:187], v[32:35]\n\t"
  "ds_read_b128 v[116:119], %[ac1] offset:8704\n\t"
  "v_mfma_f32_16x16x32_bf16 v[36:39], v[96:99], v[188:191], v[36:39]\n\t"
  "v_mfma_f32_16x16x32_bf16 v[40:43], v[100:103], v[184:187], v[40:43]\n\t"
  "ds_read_b128 v[120:123], %[ac1] offset:12288\n\t"
  "v_mfma_f32_16x16x32_bf16 v[44:47], v[100:103], v[188:191], v[44:47]\n\t"
  "s_waitcnt lgkmcnt(3)\n\t"
  "v_mfma_f32_16x16x32_bf16 v[48:51], v[104:107], v[184:187], v[48:51]\n\t"
  "ds_read_b128 v[124:127], %[ac1] offset:12800\n\t"
  "v_mfma_f32_16x16x32_bf16 v[52:55], v[104:107], v[188:191], v[52:55]\n\t"
  "v_mfma_f32_16x16x32_bf16 v[56:59], v[108:111], v[184:187], v[56:59]\n\t"
  "v_mfma_f32_16x16x32_bf16 v[60:63], v[108:111], v[188:191], v[60:63]\n\t"
  "s_waitcnt lgkmcnt(2)\n\t"
  "v_mfma_f32_16x16x32_bf16 v[64:67], v[112:115], v[184:187], v[64:67]\n\t"
  "v_mfma_f32_16x16x32_bf16 v[68:71], v[112:115], v[188:191], v[68:71]\n\t"
  "v_mfma_f32_16x16x32_bf16 v[72:75], v[116:119], v[184:187], v[72:75]\n\t"
  "v_mfma_f32_16x16x32_bf16 v[76:79], v[116:119], v[188:191], v[76:79]\n\t"
  "s_waitcnt lgkmcnt(0)\n\t"
  "v_mfma_f32_16x16x32_bf16 v[80:83], v[120:123], v[184:187], v[80:83]\n\t"
  "v_mfma_f32_16x16x32_bf16 v[84:87], v[120:123], v[188:191], v[84:87]\n\t"
  "v_mfma_f32_16x16x32_bf16 v[88:91], v[124:127], v[184:187], v[88:91]\n\t"
  "v_mfma_f32_16x16x32_bf16 v[92:95], v[124:127], v[188:191], v[92:95]\n\t"
  "s_waitcnt vmcnt(0)\n\t"
  "s_barrier\n\t"
  : [ax0]"+v"(ax[0]),[ax1]"+v"(ax[1]),[ax2]"+v"(ax[2]),[ax3]"+v"(ax[3]),[ac0]"+v"(ac[0]),[ac1]"+v"(ac[1]),[ac2]"+v"(ac[2]),[ac3]"+v"(ac[3]),[cnt]"=&s"(scnt),[msk]"=&s"(smask),[ep]"=&s"(tag),"=&{v[32:47]}"(y0),"=&{v[48:63]}"(y1),"=&{v[64:79]}"(y2),"=&{v[80:95]}"(y3)
  : [vl]"v"(vl),[axw]"v"(axw),[awo]"v"(awo),[neg]"s"(neg),[pq0]"s"(pq0),[pq1]"s"(pq1),[epp]"s"(ws+WS_EP)
  : "memory","scc","vcc","v96","v97","v98","v99","v100","v101","v102","v103","v104","v105","v106","v107","v108","v109","v110","v111","v112","v113","v114","v115","v116","v117","v118","v119","v120","v121","v122","v123","v124","v125","v126","v127","v128","v129","v130","v131","v132","v133","v134","v135","v136","v137","v138","v139","v140","v141","v142","v143","v144","v145","v146","v147","v148","v149","v150","v151","v152","v153","v154","v155","v156","v157","v158","v159","v160","v161","v162","v163","v164","v165","v166","v167","v168","v169","v170","v171","v172","v173","v174","v175","v176","v177","v178","v179","v180","v181","v182","v183","v184","v185","v186","v187","v188","v189","v190","v191","v192","v193","v194","v195","v196","v197","v198","v199","v200","v201","v202","v203","v204","v205","v206","v207","v208","v209","v210","v211","v212","v213","v214","v215","v216","v217","v218","v219","v220","v221","v222","v223","v224","v225","v226","v227","v228","v229","v230","v231","v232","v233","v234","v235","v236","v237","v238","v239","v240","v241","v242","v243","v244","v245","v246","v247","v248","v249","v250","v251","v252","v253","v254","v255");

      tag+=1;
      if(wid==0 && lane<40) st16_dev(fvo,fl,tag4(tag));
      if(wid==1){
        __builtin_amdgcn_s_sleep(POLL_DELAY);
        int n=0;
        #pragma unroll 1
        for(;n<SPIN;++n){ if(__builtin_amdgcn_ballot_w64(ld8_dev(pvo,fl)!=tag)==0) break; }
      }

asm volatile(
  "s_barrier\n\t"
  "global_load_dwordx4 v[128:131], %[vl], %[pb0] offset:-4096 sc1\n\t"
  "global_load_dwordx4 v[132:135], %[vl], %[pb0] offset:-3072 sc1\n\t"
  "global_load_dwordx4 v[136:139], %[vl], %[pb0] offset:-2048 sc1\n\t"
  "global_load_dwordx4 v[140:143], %[vl], %[pb0] offset:-1024 sc1\n\t"
  "global_load_dwordx4 v[144:147], %[vl], %[pb1] offset:-4096 sc1\n\t"
  "global_load_dwordx4 v[148:151], %[vl], %[pb1] offset:-3072 sc1\n\t"
  "global_load_dwordx4 v[152:155], %[vl], %[pb1] offset:-2048 sc1\n\t"
  "global_load_dwordx4 v[156:159], %[vl], %[pb1] offset:-1024 sc1\n\t"
  "v_xor_b32 %[ac0], 0x10000, %[aw]\n\t"
  "ds_read_b128 v[96:99], %[ac0] offset:0\n\t"
  "ds_read_b128 v[100:103], %[ac0] offset:512\n\t"
  "ds_read_b128 v[104:107], %[ac0] offset:4096\n\t"
  "ds_read_b128 v[108:111], %[ac0] offset:4608\n\t"
  "ds_read_b128 v[112:115], %[ac0] offset:8192\n\t"
  "s_waitcnt vmcnt(6)\n\t"
  "s_waitcnt lgkmcnt(3)\n\t"
  "v_mfma_f32_16x16x32_bf16 v[32:35], v[96:99], v[128:131], v[32:35]\n\t"
  "ds_read_b128 v[116:119], %[ac0] offset:8704\n\t"
  "v_mfma_f32_16x16x32_bf16 v[36:39], v[96:99], v[132:135], v[36:39]\n\t"
  "global_load_dwordx4 v[160:163], %[vl], %[pb2] offset:-4096 sc1\n\t"
  "v_mfma_f32_16x16x32_bf16 v[40:43], v[100:103], v[128:131], v[40:43]\n\t"
  "ds_read_b128 v[120:123], %[ac0] offset:12288\n\t"
  "v_mfma_f32_16x16x32_bf16 v[44:47], v[100:103], v[132:135], v[44:47]\n\t"
  "s_waitcnt lgkmcnt(3)\n\t"
  "v_mfma_f32_16x16x32_bf16 v[48:51], v[104:107], v[128:131], v[48:51]\n\t"
  "ds_read_b128 v[124:127], %[ac0] offset:12800\n\t"
  "v_mfma_f32_16x16x32_bf16 v[52:55], v[104:107], v[132:135], v[52:55]\n\t"
  "global_load_dwordx4 v[164:167], %[vl], %[pb2] offset:-3072 sc1\n\t"
  "v_mfma_f32_16x16x32_bf16 v[56:59], v[108:111], v[128:131], v[56:59]\n\t"
  "v_xor_b32 %[ac1], 0x10040, %[aw]\n\t"
  "ds_read_b128 v[96:99], %[ac1] offset:0\n\t"
  "v_mfma_f32_16x16x32_bf16 v[60:63], v[108:111], v[132:135], v[60:63]\n\t"
  "s_waitcnt lgkmcnt(3)\n\t"
  "v_mfma_f32_16x16x32_bf16 v[64:67], v[112:115], v[128:131], v[64:67]\n\t"
  "ds_read_b128 v[100:103], %[ac1] offset:512\n\t"
  "v_mfma_f32_16x16x32_bf16 v[68:71], v[112:115], v[132:135], v[68:71]\n\t"
  "global_load_dwordx4 v[168:171], %[vl], %[pb2] offset:-2048 sc1\n\t"
  "v_mfma_f32_16x16x32_bf16 v[72:75], v[116:119], v[128:131], v[72:75]\n\t"
  "ds_read_b128 v[104:107], %[ac1] offset:4096\n\t"
  "v_mfma_f32_16x16x32_bf16 v[76:79], v[116:119], v[132:135], v[76:79]\n\t"
  "s_waitcnt lgkmcnt(3)\n\t"
  "v_mfma_f32_16x16x32_bf16 v[80:83], v[120:123], v[128:131], v[80:83]\n\t"
  "ds_read_b128 v[108:111], %[ac1] offset:4608\n\t"
  "v_mfma_f32_16x16x32_bf16 v[84:87], v[120:123], v[132:135], v[84:87]\n\t"
  "global_load_dwordx4 v[172:175], %[vl], %[pb2] offset:-1024 sc1\n\t"
  "v_mfma_f32_16x16x32_bf16 v[88:91], v[124:127], v[128:131], v[88:91]\n\t"
  "ds_read_b128 v[112:115], %[ac1] offset:8192\n\t"
  "v_mfma_f32_16x16x32_bf16 v[92:95], v[124:127], v[132:135], v[92:95]\n\t"
  "s_waitcnt vmcnt(8)\n\t"
  "s_waitcnt lgkmcnt(3)\n\t"
  "v_mfma_f32_16x16x32_bf16 v[32:35], v[96:99], v[136:139], v[32:35]\n\t"
  "ds_read_b128 v[116:119], %[ac1] offset:8704\n\t"
  "v_mfma_f32_16x16x32_bf16 v[36:39], v[96:99], v[140:143], v[36:39]\n\t"
  "global_load_dwordx4 v[176:179], %[vl], %[pb3] offset:-4096 sc1\n\t"
  "v_mfma_f32_16x16x32_bf16 v[40:43], v[100:103], v[136:139], v[40:43]\n\t"
  "ds_read_b128 v[120:123], %[ac1] offset:12288\n\t"
  "v_mfma_f32_16x16x32_bf16 v[44:47], v[100:103], v[140:143], v[44:47]\n\t"
  "s_waitcnt lgkmcnt(3)\n\t"
  "v_mfma_f32_16x16x32_bf16 v[48:51], v[104:107], v[136:139], v[48:51]\n\t"
  "ds_read_b128 v[124:127], %[ac1] offset:12800\n\t"
  "v_mfma_f32_16x16x32_bf16 v[52:55], v[104:107], v[140:143], v[52:55]\n\t"
  "global_load_dwordx4 v[180:183], %[vl], %[pb3] offset:-3072 sc1\n\t"
  "v_mfma_f32_16x16x32_bf16 v[56:59], v[108:111], v[136:139], v[56:59]\n\t"
  "v_xor_b32 %[ac0], 0x18000, %[aw]\n\t"
  "ds_read_b128 v[96:99], %[ac0] offset:0\n\t"
  "v_mfma_f32_16x16x32_bf16 v[60:63], v[108:111], v[140:143], v[60:63]\n\t"
  "s_waitcnt lgkmcnt(3)\n\t"
  "v_mfma_f32_16x16x32_bf16 v[64:67], v[112:115], v[136:139], v[64:67]\n\t"
  "ds_read_b128 v[100:103], %[ac0] offset:512\n\t"
  "v_mfma_f32_16x16x32_bf16 v[68:71], v[112:115], v[140:143], v[68:71]\n\t"
  "global_load_dwordx4 v[184:187], %[vl], %[pb3] offset:-2048 sc1\n\t"
  "v_mfma_f32_16x16x32_bf16 v[72:75], v[116:119], v[136:139], v[72:75]\n\t"
  "ds_read_b128 v[104:107], %[ac0] offset:4096\n\t"
  "v_mfma_f32_16x16x32_bf16 v[76:79], v[116:119], v[140:143], v[76:79]\n\t"
  "s_waitcnt lgkmcnt(3)\n\t"
  "v_mfma_f32_16x16x32_bf16 v[80:83], v[120:123], v[136:139], v[80:83]\n\t"
  "ds_read_b128 v[108:111], %[ac0] offset:4608\n\t"
  "v_mfma_f32_16x16x32_bf16 v[84:87], v[120:123], v[140:143], v[84:87]\n\t"
  "global_load_dwordx4 v[188:191], %[vl], %[pb3] offset:-1024 sc1\n\t"
  "v_mfma_f32_16x16x32_bf16 v[88:91], v[124:127], v[136:139], v[88:91]\n\t"
  "ds_read_b128 v[112:115], %[ac0] offset:8192\n\t"
  "v_mfma_f32_16x16x32_bf16 v[92:95], v[124:127], v[140:143], v[92:95]\n\t"
  "s_waitcnt vmcnt(10)\n\t"
  "s_waitcnt lgkmcnt(3)\n\t"
  "v_mfma_f32_16x16x32_bf16 v[32:35], v[96:99], v[144:147], v[32:35]\n\t"
  "ds_read_b128 v[116:119], %[ac0] offset:8704\n\t"
  "v_mfma_f32_16x16x32_bf16 v[36:39], v[96:99], v[148:151], v[36:39]\n\t"
  "global_load_dwordx4 v[192:195], %[vl], %[pb0] offset:0 sc1\n\t"
  "v_mfma_f32_16x16x32_bf16 v[40:43], v[100:103], v[144:147], v[40:43]\n\t"
  "ds_read_b128 v[120:123], %[ac0] offset:12288\n\t"
  "v_mfma_f32_16x16x32_bf16 v[44:47], v[100:103], v[148:151], v[44:47]\n\t"
  "s_waitcnt lgkmcnt(3)\n\t"
  "v_mfma_f32_16x16x32_bf16 v[48:51], v[104:107], v[144:147], v[48:51]\n\t"
  "ds_read_b128 v[124:127], %[ac0] offset:12800\n\t"
  "v_mfma_f32_16x16x32_bf16 v[52:55], v[104:107], v[148:151], v[52:55]\n\t"
  "global_load_dwordx4 v[196:199], %[vl], %[pb0] offset:1024 sc1\n\t"
  "v_mfma_f32_16x16x32_bf16 v[56:59], v[108:111], v[144:147], v[56:59]\n\t"
  "v_xor_b32 %[ac1], 0x18040, %[aw]\n\t"
  "ds_read_b128 v[96:99], %[ac1] offset:0\n\t"
  "v_mfma_f32_16x16x32_bf16 v[60:63], v[108:111], v[148:151], v[60:63]\n\t"
  "s_waitcnt lgkmcnt(3)\n\t"
  "v_mfma_f32_16x16x32_bf16 v[64:67], v[112:115], v[144:147], v[64:67]\n\t"
  "ds_read_b128 v[100:103], %[ac1] offset:512\n\t"
  "v_mfma_f32_16x16x32_bf16 v[68:71], v[112:115], v[148:151], v[68:71]\n\t"
  "global_load_dwordx4 v[200:203], %[vl], %[pb0] offset:2048 sc1\n\t"
  "v_mfma_f32_16x16x32_bf16 v[72:75], v[116:119], v[144:147], v[72:75]\n\t"
  "ds_read_b128 v[104:107], %[ac1] offset:4096\n\t"
  "v_mfma_f32_16x16x32_bf16 v[76:79], v[116:119], v[148:151], v[76:79]\n\t"
  "s_waitcnt lgkmcnt(3)\n\t"
  "v_mfma_f32_16x16x32_bf16 v[80:83], v[120:123], v[144:147], v[80:83]\n\t"
  "ds_read_b128 v[108:111], %[ac1] offset:4608\n\t"
  "v_mfma_f32_16x16x32_bf16 v[84:87], v[120:123], v[148:151], v[84:87]\n\t"
  "global_load_dwordx4 v[204:207], %[vl], %[pb0] offset:3072 sc1\n\t"
  "v_mfma_f32_16x16x32_bf16 v[88:91], v[124:127], v[144:147], v[88:91]\n\t"
  "ds_read_b128 v[112:115], %[ac1] offset:8192\n\t"
  "v_mfma_f32_16x16x32_bf16 v[92:95], v[124:127], v[148:151], v[92:95]\n\t"
  "s_waitcnt vmcnt(12)\n\t"
  "s_waitcnt lgkmcnt(3)\n\t"
  "v_mfma_f32_16x16x32_bf16 v[32:35], v[96:99], v[152:155], v[32:35]\n\t"
  "ds_read_b128 v[116:119], %[ac1] offset:8704\n\t"
  "v_mfma_f32_16x16x32_bf16 v[36:39], v[96:99], v[156:159], v[36:39]\n\t"
  "global_load_dwordx4 v[208:211], %[vl], %[pb1] offset:0 sc1\n\t"
  "v_mfma_f32_16x16x32_bf16 v[40:43], v[100:103], v[152:155], v[40:43]\n\t"
  "ds_read_b128 v[120:123], %[ac1] offset:12288\n\t"
  "v_mfma_f32_16x16x32_bf16 v[44:47], v[100:103], v[156:159], v[44:47]\n\t"
  "s_waitcnt lgkmcnt(3)\n\t"
  "v_mfma_f32_16x16x32_bf16 v[48:51], v[104:107], v[152:155], v[48:51]\n\t"
  "ds_read_b128 v[124:127], %[ac1] offset:12800\n\t"
  "v_mfma_f32_16x16x32_bf16 v[52:55], v[104:107], v[156:159], v[52:55]\n\t"
  "global_load_dwordx4 v[212:215], %[vl], %[pb1] offset:1024 sc1\n\t"
  "v_mfma_f32_16x16x32_bf16 v[56:59], v[108:111], v[152:155], v[56:59]\n\t"
  "s_barrier\n\t"
  "v_xor_b32 %[ac0], 0x20000, %[aw]\n\t"
  "ds_read_b128 v[96:99], %[ac0] offset:0\n\t"
  "v_mfma_f32_16x16x32_bf16 v[60:63], v[108:111], v[156:159], v[60:63]\n\t"
  "s_waitcnt lgkmcnt(3)\n\t"
  "v_mfma_f32_16x16x32_bf16 v[64:67], v[112:115], v[152:155], v[64:67]\n\t"
  "ds_read_b128 v[100:103], %[ac0] offset:512\n\t"
  "v_mfma_f32_16x16x32_bf16 v[68:71], v[112:115], v[156:159], v[68:71]\n\t"
  "global_load_dwordx4 v[216:219], %[vl], %[pb1] offset:2048 sc1\n\t"
  "v_mfma_f32_16x16x32_bf16 v[72:75], v[116:119], v[152:155], v[72:75]\n\t"
  "ds_read_b128 v[104:107], %[ac0] offset:4096\n\t"
  "v_mfma_f32_16x16x32_bf16 v[76:79], v[116:119], v[156:159], v[76:79]\n\t"
  "s_waitcnt lgkmcnt(3)\n\t"
  "v_mfma_f32_16x16x32_bf16 v[80:83], v[120:123], v[152:155], v[80:83]\n\t"
  "ds_read_b128 v[108:111], %[ac0] offset:4608\n\t"
  "v_mfma_f32_16x16x32_bf16 v[84:87], v[120:123], v[156:159], v[84:87]\n\t"
  "global_load_dwordx4 v[220:223], %[vl], %[pb1] offset:3072 sc1\n\t"
  "v_mfma_f32_16x16x32_bf16 v[88:91], v[124:127], v[152:155], v[88:91]\n\t"
  "ds_read_b128 v[112:115], %[ac0] offset:8192\n\t"
  "v_mfma_f32_16x16x32_bf16 v[92:95], v[124:127], v[156:159], v[92:95]\n\t"
  "s_waitcnt vmcnt(14)\n\t"
  "s_waitcnt lgkmcnt(3)\n\t"
  "v_mfma_f32_16x16x32_bf16 v[32:35], v[96:99], v[160:163], v[32:35]\n\t"
  "ds_read_b128 v[116:119], %[ac0] offset:8704\n\t"
  "v_mfma_f32_16x16x32_bf16 v[36:39], v[96:99], v[164:167], v[36:39]\n\t"
  "global_load_dwordx4 v[224:227], %[vl], %[pb2] offset:0 sc1\n\t"
  "v_mfma_f32_16x16x32_bf16 v[40:43], v[100:103], v[160:163], v[40:43]\n\t"
  "ds_read_b128 v[120:123], %[ac0] offset:12288\n\t"
  "v_mfma_f32_16x16x32_bf16 v[44:47], v[100:103], v[164:167], v[44:47]\n\t"
  "s_waitcnt lgkmcnt(3)\n\t"
  "v_mfma_f32_16x16x32_bf16 v[48:51], v[104:107], v[160:163], v[48:51]\n\t"
  "ds_read_b128 v[124:127], %[ac0] offset:12800\n\t"
  "v_mfma_f32_16x16x32_bf16 v[52:55], v[104:107], v[164:167], v[52:55]\n\t"
  "global_load_dwordx4 v[228:231], %[vl], %[pb2] offset:1024 sc1\n\t"
  "v_mfma_f32_16x16x32_bf16 v[56:59], v[108:111], v[160:163], v[56:59]\n\t"
  "v_xor_b32 %[ac1], 0x20040, %[aw]\n\t"
  "ds_read_b128 v[96:99], %[ac1] offset:0\n\t"
  "v_mfma_f32_16x16x32_bf16 v[60:63], v[108:111], v[164:167], v[60:63]\n\t"
  "s_waitcnt lgkmcnt(3)\n\t"
  "v_mfma_f32_16x16x32_bf16 v[64:67], v[112:115], v[160:163], v[64:67]\n\t"
  "ds_read_b128 v[100:103], %[ac1] offset:512\n\t"
  "v_mfma_f32_16x16x32_bf16 v[68:71], v[112:115], v[164:167], v[68:71]\n\t"
  "global_load_dwordx4 v[232:235], %[vl], %[pb2] offset:2048 sc1\n\t"
  "v_mfma_f32_16x16x32_bf16 v[72:75], v[116:119], v[160:163], v[72:75]\n\t"
  "ds_read_b128 v[104:107], %[ac1] offset:4096\n\t"
  "v_mfma_f32_16x16x32_bf16 v[76:79], v[116:119], v[164:167], v[76:79]\n\t"
  "s_waitcnt lgkmcnt(3)\n\t"
  "v_mfma_f32_16x16x32_bf16 v[80:83], v[120:123], v[160:163], v[80:83]\n\t"
  "ds_read_b128 v[108:111], %[ac1] offset:4608\n\t"
  "v_mfma_f32_16x16x32_bf16 v[84:87], v[120:123], v[164:167], v[84:87]\n\t"
  "global_load_dwordx4 v[236:239], %[vl], %[pb2] offset:3072 sc1\n\t"
  "v_mfma_f32_16x16x32_bf16 v[88:91], v[124:127], v[160:163], v[88:91]\n\t"
  "ds_read_b128 v[112:115], %[ac1] offset:8192\n\t"
  "v_mfma_f32_16x16x32_bf16 v[92:95], v[124:127], v[164:167], v[92:95]\n\t"
  "s_waitcnt vmcnt(16)\n\t"
  "s_waitcnt lgkmcnt(3)\n\t"
  "v_mfma_f32_16x16x32_bf16 v[32:35], v[96:99], v[168:171], v[32:35]\n\t"
  "ds_read_b128 v[116:119], %[ac1] offset:8704\n\t"
  "v_mfma_f32_16x16x32_bf16 v[36:39], v[96:99], v[172:175], v[36:39]\n\t"
  "global_load_dwordx4 v[240:243], %[vl], %[pb3] offset:0 sc1\n\t"
  "v_mfma_f32_16x16x32_bf16 v[40:43], v[100:103], v[168:171], v[40:43]\n\t"
  "ds_read_b128 v[120:123], %[ac1] offset:12288\n\t"
  "v_mfma_f32_16x16x32_bf16 v[44:47], v[100:103], v[172:175], v[44:47]\n\t"
  "s_waitcnt lgkmcnt(3)\n\t"
  "v_mfma_f32_16x16x32_bf16 v[48:51], v[104:107], v[168:171], v[48:51]\n\t"
  "ds_read_b128 v[124:127], %[ac1] offset:12800\n\t"
  "v_mfma_f32_16x16x32_bf16 v[52:55], v[104:107], v[172:175], v[52:55]\n\t"
  "global_load_dwordx4 v[244:247], %[vl], %[pb3] offset:1024 sc1\n\t"
  "v_mfma_f32_16x16x32_bf16 v[56:59], v[108:111], v[168:171], v[56:59]\n\t"
  "s_barrier\n\t"
  "v_xor_b32 %[ac0], 0x0, %[aw]\n\t"
  "ds_read_b128 v[96:99], %[ac0] offset:0\n\t"
  "v_mfma_f32_16x16x32_bf16 v[60:63], v[108:111], v[172:175], v[60:63]\n\t"
  "s_waitcnt lgkmcnt(3)\n\t"
  "v_mfma_f32_16x16x32_bf16 v[64:67], v[112:115], v[168:171], v[64:67]\n\t"
  "ds_read_b128 v[100:103], %[ac0] offset:512\n\t"
  "v_mfma_f32_16x16x32_bf16 v[68:71], v[112:115], v[172:175], v[68:71]\n\t"
  "global_load_dwordx4 v[248:251], %[vl], %[pb3] offset:2048 sc1\n\t"
  "v_mfma_f32_16x16x32_bf16 v[72:75], v[116:119], v[168:171], v[72:75]\n\t"
  "ds_read_b128 v[104:107], %[ac0] offset:4096\n\t"
  "v_mfma_f32_16x16x32_bf16 v[76:79], v[116:119], v[172:175], v[76:79]\n\t"
  "s_waitcnt lgkmcnt(3)\n\t"
  "v_mfma_f32_16x16x32_bf16 v[80:83], v[120:123], v[168:171], v[80:83]\n\t"
  "ds_read_b128 v[108:111], %[ac0] offset:4608\n\t"
  "v_mfma_f32_16x16x32_bf16 v[84:87], v[120:123], v[172:175], v[84:87]\n\t"
  "global_load_dwordx4 v[252:255], %[vl], %[pb3] offset:3072 sc1\n\t"
  "v_mfma_f32_16x16x32_bf16 v[88:91], v[124:127], v[168:171], v[88:91]\n\t"
  "ds_read_b128 v[112:115], %[ac0] offset:8192\n\t"
  "v_mfma_f32_16x16x32_bf16 v[92:95], v[124:127], v[172:175], v[92:95]\n\t"
  "s_waitcnt vmcnt(18)\n\t"
  "s_waitcnt lgkmcnt(3)\n\t"
  "v_mfma_f32_16x16x32_bf16 v[32:35], v[96:99], v[176:179], v[32:35]\n\t"
  "ds_read_b128 v[116:119], %[ac0] offset:8704\n\t"
  "v_mfma_f32_16x16x32_bf16 v[36:39], v[96:99], v[180:183], v[36:39]\n\t"
  "v_mfma_f32_16x16x32_bf16 v[40:43], v[100:103], v[176:179], v[40:43]\n\t"
  "ds_read_b128 v[120:123], %[ac0] offset:12288\n\t"
  "v_mfma_f32_16x16x32_bf16 v[44:47], v[100:103], v[180:183], v[44:47]\n\t"
  "s_waitcnt lgkmcnt(3)\n\t"
  "v_mfma_f32_16x16x32_bf16 v[48:51], v[104:107], v[176:179], v[48:51]\n\t"
  "ds_read_b128 v[124:127], %[ac0] offset:12800\n\t"
  "v_mfma_f32_16x16x32_bf16 v[52:55], v[104:107], v[180:183], v[52:55]\n\t"
  "v_mfma_f32_16x16x32_bf16 v[56:59], v[108:111], v[176:179], v[56:59]\n\t"
  "v_xor_b32 %[ac1], 0x40, %[aw]\n\t"
  "ds_read_b128 v[96:99], %[ac1] offset:0\n\t"
  "v_mfma_f32_16x16x32_bf16 v[60:63], v[108:111], v[180:183], v[60:63]\n\t"
  "s_waitcnt lgkmcnt(3)\n\t"
  "v_mfma_f32_16x16x32_bf16 v[64:67], v[112:115], v[176:179], v[64:67]\n\t"
  "ds_read_b128 v[100:103], %[ac1] offset:512\n\t"
  "v_mfma_f32_16x16x32_bf16 v[68:71], v[112:115], v[180:183], v[68:71]\n\t"
  "v_mfma_f32_16x16x32_bf16 v[72:75], v[116:119], v[176:179], v[72:75]\n\t"
  "ds_read_b128 v[104:107], %[ac1] offset:4096\n\t"
  "v_mfma_f32_16x16x32_bf16 v[76:79], v[116:119], v[180:183], v[76:79]\n\t"
  "s_waitcnt lgkmcnt(3)\n\t"
  "v_mfma_f32_16x16x32_bf16 v[80:83], v[120:123], v[176:179], v[80:83]\n\t"
  "ds_read_b128 v[108:111], %[ac1] offset:4608\n\t"
  "v_mfma_f32_16x16x32_bf16 v[84:87], v[120:123], v[180:183], v[84:87]\n\t"
  "v_mfma_f32_16x16x32_bf16 v[88:91], v[124:127], v[176:179], v[88:91]\n\t"
  "ds_read_b128 v[112:115], %[ac1] offset:8192\n\t"
  "v_mfma_f32_16x16x32_bf16 v[92:95], v[124:127], v[180:183], v[92:95]\n\t"
  "s_waitcnt vmcnt(16)\n\t"
  "s_waitcnt lgkmcnt(3)\n\t"
  "v_mfma_f32_16x16x32_bf16 v[32:35], v[96:99], v[184:187], v[32:35]\n\t"
  "ds_read_b128 v[116:119], %[ac1] offset:8704\n\t"
  "v_mfma_f32_16x16x32_bf16 v[36:39], v[96:99], v[188:191], v[36:39]\n\t"
  "v_mfma_f32_16x16x32_bf16 v[40:43], v[100:103], v[184:187], v[40:43]\n\t"
  "ds_read_b128 v[120:123], %[ac1] offset:12288\n\t"
  "v_mfma_f32_16x16x32_bf16 v[44:47], v[100:103], v[188:191], v[44:47]\n\t"
  "s_waitcnt lgkmcnt(3)\n\t"
  "v_mfma_f32_16x16x32_bf16 v[48:51], v[104:107], v[184:187], v[48:51]\n\t"
  "ds_read_b128 v[124:127], %[ac1] offset:12800\n\t"
  "v_mfma_f32_16x16x32_bf16 v[52:55], v[104:107], v[188:191], v[52:55]\n\t"
  "v_mfma_f32_16x16x32_bf16 v[56:59], v[108:111], v[184:187], v[56:59]\n\t"
  "s_barrier\n\t"
  "v_xor_b32 %[ac0], 0x8000, %[aw]\n\t"
  "ds_read_b128 v[96:99], %[ac0] offset:0\n\t"
  "v_mfma_f32_16x16x32_bf16 v[60:63], v[108:111], v[188:191], v[60:63]\n\t"
  "s_waitcnt lgkmcnt(3)\n\t"
  "v_mfma_f32_16x16x32_bf16 v[64:67], v[112:115], v[184:187], v[64:67]\n\t"
  "ds_read_b128 v[100:103], %[ac0] offset:512\n\t"
  "v_mfma_f32_16x16x32_bf16 v[68:71], v[112:115], v[188:191], v[68:71]\n\t"
  "v_mfma_f32_16x16x32_bf16 v[72:75], v[116:119], v[184:187], v[72:75]\n\t"
  "ds_read_b128 v[104:107], %[ac0] offset:4096\n\t"
  "v_mfma_f32_16x16x32_bf16 v[76:79], v[116:119], v[188:191], v[76:79]\n\t"
  "s_waitcnt lgkmcnt(3)\n\t"
  "v_mfma_f32_16x16x32_bf16 v[80:83], v[120:123], v[184:187], v[80:83]\n\t"
  "ds_read_b128 v[108:111], %[ac0] offset:4608\n\t"
  "v_mfma_f32_16x16x32_bf16 v[84:87], v[120:123], v[188:191], v[84:87]\n\t"
  "v_mfma_f32_16x16x32_bf16 v[88:91], v[124:127], v[184:187], v[88:91]\n\t"
  "ds_read_b128 v[112:115], %[ac0] offset:8192\n\t"
  "v_mfma_f32_16x16x32_bf16 v[92:95], v[124:127], v[188:191], v[92:95]\n\t"
  "s_waitcnt vmcnt(14)\n\t"
  "s_waitcnt lgkmcnt(3)\n\t"
  "v_mfma_f32_16x16x32_bf16 v[32:35], v[96:99], v[192:195], v[32:35]\n\t"
  "ds_read_b128 v[116:119], %[ac0] offset:8704\n\t"
  "v_mfma_f32_16x16x32_bf16 v[36:39], v[96:99], v[196:199], v[36:39]\n\t"
  "v_mfma_f32_16x16x32_bf16 v[40:43], v[100:103], v[192:195], v[40:43]\n\t"
  "ds_read_b128 v[120:123], %[ac0] offset:12288\n\t"
  "v_mfma_f32_16x16x32_bf16 v[44:47], v[100:103], v[196:199], v[44:47]\n\t"
  "s_waitcnt lgkmcnt(3)\n\t"
  "v_mfma_f32_16x16x32_bf16 v[48:51], v[104:107], v[192:195], v[48:51]\n\t"
  "ds_read_b128 v[124:127], %[ac0] offset:12800\n\t"
  "v_mfma_f32_16x16x32_bf16 v[52:55], v[104:107], v[196:199], v[52:55]\n\t"
  "v_mfma_f32_16x16x32_bf16 v[56:59], v[108:111], v[192:195], v[56:59]\n\t"
  "v_xor_b32 %[ac1], 0x8040, %[aw]\n\t"
  "ds_read_b128 v[96:99], %[ac1] offset:0\n\t"
  "v_mfma_f32_16x16x32_bf16 v[60:63], v[108:111], v[196:199], v[60:63]\n\t"
  "s_waitcnt lgkmcnt(3)\n\t"
  "v_mfma_f32_16x16x32_bf16 v[64:67], v[112:115], v[192:195], v[64:67]\n\t"
  "ds_read_b128 v[100:103], %[ac1] offset:512\n\t"
  "v_mfma_f32_16x16x32_bf16 v[68:71], v[112:115], v[196:199], v[68:71]\n\t"
  "v_mfma_f32_16x16x32_bf16 v[72:75], v[116:119], v[192:195], v[72:75]\n\t"
  "ds_read_b128 v[104:107], %[ac1] offset:4096\n\t"
  "v_mfma_f32_16x16x32_bf16 v[76:79], v[116:119], v[196:199], v[76:79]\n\t"
  "s_waitcnt lgkmcnt(3)\n\t"
  "v_mfma_f32_16x16x32_bf16 v[80:83], v[120:123], v[192:195], v[80:83]\n\t"
  "ds_read_b128 v[108:111], %[ac1] offset:4608\n\t"
  "v_mfma_f32_16x16x32_bf16 v[84:87], v[120:123], v[196:199], v[84:87]\n\t"
  "v_mfma_f32_16x16x32_bf16 v[88:91], v[124:127], v[192:195], v[88:91]\n\t"
  "ds_read_b128 v[112:115], %[ac1] offset:8192\n\t"
  "v_mfma_f32_16x16x32_bf16 v[92:95], v[124:127], v[196:199], v[92:95]\n\t"
  "s_waitcnt vmcnt(12)\n\t"
  "s_waitcnt lgkmcnt(3)\n\t"
  "v_mfma_f32_16x16x32_bf16 v[32:35], v[96:99], v[200:203], v[32:35]\n\t"
  "ds_read_b128 v[116:119], %[ac1] offset:8704\n\t"
  "v_mfma_f32_16x16x32_bf16 v[36:39], v[96:99], v[204:207], v[36:39]\n\t"
  "v_mfma_f32_16x16x32_bf16 v[40:43], v[100:103], v[200:203], v[40:43]\n\t"
  "ds_read_b128 v[120:123], %[ac1] offset:12288\n\t"
  "v_mfma_f32_16x16x32_bf16 v[44:47], v[100:103], v[204:207], v[44:47]\n\t"
  "s_waitcnt lgkmcnt(3)\n\t"
  "v_mfma_f32_16x16x32_bf16 v[48:51], v[104:107], v[200:203], v[48:51]\n\t"
  "ds_read_b128 v[124:127], %[ac1] offset:12800\n\t"
  "v_mfma_f32_16x16x32_bf16 v[52:55], v[104:107], v[204:207], v[52:55]\n\t"
  "v_mfma_f32_16x16x32_bf16 v[56:59], v[108:111], v[200:203], v[56:59]\n\t"
  "s_barrier\n\t"
  "v_xor_b32 %[ac0], 0x10000, %[aw]\n\t"
  "ds_read_b128 v[96:99], %[ac0] offset:0\n\t"
  "v_mfma_f32_16x16x32_bf16 v[60:63], v[108:111], v[204:207], v[60:63]\n\t"
  "s_waitcnt lgkmcnt(3)\n\t"
  "v_mfma_f32_16x16x32_bf16 v[64:67], v[112:115], v[200:203], v[64:67]\n\t"
  "ds_read_b128 v[100:103], %[ac0] offset:512\n\t"
  "v_mfma_f32_16x16x32_bf16 v[68:71], v[112:115], v[204:207], v[68:71]\n\t"
  "v_mfma_f32_16x16x32_bf16 v[72:75], v[116:119], v[200:203], v[72:75]\n\t"
  "ds_read_b128 v[104:107], %[ac0] offset:4096\n\t"
  "v_mfma_f32_16x16x32_bf16 v[76:79], v[116:119], v[204:207], v[76:79]\n\t"
  "s_waitcnt lgkmcnt(3)\n\t"
  "v_mfma_f32_16x16x32_bf16 v[80:83], v[120:123], v[200:203], v[80:83]\n\t"
  "ds_read_b128 v[108:111], %[ac0] offset:4608\n\t"
  "v_mfma_f32_16x16x32_bf16 v[84:87], v[120:123], v[204:207], v[84:87]\n\t"
  "v_mfma_f32_16x16x32_bf16 v[88:91], v[124:127], v[200:203], v[88:91]\n\t"
  "ds_read_b128 v[112:115], %[ac0] offset:8192\n\t"
  "v_mfma_f32_16x16x32_bf16 v[92:95], v[124:127], v[204:207], v[92:95]\n\t"
  "s_waitcnt vmcnt(10)\n\t"
  "s_waitcnt lgkmcnt(3)\n\t"
  "v_mfma_f32_16x16x32_bf16 v[32:35], v[96:99], v[208:211], v[32:35]\n\t"
  "ds_read_b128 v[116:119], %[ac0] offset:8704\n\t"
  "v_mfma_f32_16x16x32_bf16 v[36:39], v[96:99], v[212:215], v[36:39]\n\t"
  "v_mfma_f32_16x16x32_bf16 v[40:43], v[100:103], v[208:211], v[40:43]\n\t"
  "ds_read_b128 v[120:123], %[ac0] offset:12288\n\t"
  "v_mfma_f32_16x16x32_bf16 v[44:47], v[100:103], v[212:215], v[44:47]\n\t"
  "s_waitcnt lgkmcnt(3)\n\t"
  "v_mfma_f32_16x16x32_bf16 v[48:51], v[104:107], v[208:211], v[48:51]\n\t"
  "ds_read_b128 v[124:127], %[ac0] offset:12800\n\t"
  "v_mfma_f32_16x16x32_bf16 v[52:55], v[104:107], v[212:215], v[52:55]\n\t"
  "v_mfma_f32_16x16x32_bf16 v[56:59], v[108:111], v[208:211], v[56:59]\n\t"
  "v_xor_b32 %[ac1], 0x10040, %[aw]\n\t"
  "ds_read_b128 v[96:99], %[ac1] offset:0\n\t"
  "v_mfma_f32_16x16x32_bf16 v[60:63], v[108:111], v[212:215], v[60:63]\n\t"
  "s_waitcnt lgkmcnt(3)\n\t"
  "v_mfma_f32_16x16x32_bf16 v[64:67], v[112:115], v[208:211], v[64:67]\n\t"
  "ds_read_b128 v[100:103], %[ac1] offset:512\n\t"
  "v_mfma_f32_16x16x32_bf16 v[68:71], v[112:115], v[212:215], v[68:71]\n\t"
  "v_mfma_f32_16x16x32_bf16 v[72:75], v[116:119], v[208:211], v[72:75]\n\t"
  "ds_read_b128 v[104:107], %[ac1] offset:4096\n\t"
  "v_mfma_f32_16x16x32_bf16 v[76:79], v[116:119], v[212:215], v[76:79]\n\t"
  "s_waitcnt lgkmcnt(3)\n\t"
  "v_mfma_f32_16x16x32_bf16 v[80:83], v[120:123], v[208:211], v[80:83]\n\t"
  "ds_read_b128 v[108:111], %[ac1] offset:4608\n\t"
  "v_mfma_f32_16x16x32_bf16 v[84:87], v[120:123], v[212:215], v[84:87]\n\t"
  "v_mfma_f32_16x16x32_bf16 v[88:91], v[124:127], v[208:211], v[88:91]\n\t"
  "ds_read_b128 v[112:115], %[ac1] offset:8192\n\t"
  "v_mfma_f32_16x16x32_bf16 v[92:95], v[124:127], v[212:215], v[92:95]\n\t"
  "s_waitcnt vmcnt(8)\n\t"
  "s_waitcnt lgkmcnt(3)\n\t"
  "v_mfma_f32_16x16x32_bf16 v[32:35], v[96:99], v[216:219], v[32:35]\n\t"
  "ds_read_b128 v[116:119], %[ac1] offset:8704\n\t"
  "v_mfma_f32_16x16x32_bf16 v[36:39], v[96:99], v[220:223], v[36:39]\n\t"
  "v_mfma_f32_16x16x32_bf16 v[40:43], v[100:103], v[216:219], v[40:43]\n\t"
  "ds_read_b128 v[120:123], %[ac1] offset:12288\n\t"
  "v_mfma_f32_16x16x32_bf16 v[44:47], v[100:103], v[220:223], v[44:47]\n\t"
  "s_waitcnt lgkmcnt(3)\n\t"
  "v_mfma_f32_16x16x32_bf16 v[48:51], v[104:107], v[216:219], v[48:51]\n\t"
  "ds_read_b128 v[124:127], %[ac1] offset:12800\n\t"
  "v_mfma_f32_16x16x32_bf16 v[52:55], v[104:107], v[220:223], v[52:55]\n\t"
  "v_mfma_f32_16x16x32_bf16 v[56:59], v[108:111], v[216:219], v[56:59]\n\t"
  "s_barrier\n\t"
  "v_xor_b32 %[ac0], 0x18000, %[aw]\n\t"
  "ds_read_b128 v[96:99], %[ac0] offset:0\n\t"
  "v_mfma_f32_16x16x32_bf16 v[60:63], v[108:111], v[220:223], v[60:63]\n\t"
  "s_waitcnt lgkmcnt(3)\n\t"
  "v_mfma_f32_16x16x32_bf16 v[64:67], v[112:115], v[216:219], v[64:67]\n\t"
  "ds_read_b128 v[100:103], %[ac0] offset:512\n\t"
  "v_mfma_f32_16x16x32_bf16 v[68:71], v[112:115], v[220:223], v[68:71]\n\t"
  "v_mfma_f32_16x16x32_bf16 v[72:75], v[116:119], v[216:219], v[72:75]\n\t"
  "ds_read_b128 v[104:107], %[ac0] offset:4096\n\t"
  "v_mfma_f32_16x16x32_bf16 v[76:79], v[116:119], v[220:223], v[76:79]\n\t"
  "s_waitcnt lgkmcnt(3)\n\t"
  "v_mfma_f32_16x16x32_bf16 v[80:83], v[120:123], v[216:219], v[80:83]\n\t"
  "ds_read_b128 v[108:111], %[ac0] offset:4608\n\t"
  "v_mfma_f32_16x16x32_bf16 v[84:87], v[120:123], v[220:223], v[84:87]\n\t"
  "v_mfma_f32_16x16x32_bf16 v[88:91], v[124:127], v[216:219], v[88:91]\n\t"
  "ds_read_b128 v[112:115], %[ac0] offset:8192\n\t"
  "v_mfma_f32_16x16x32_bf16 v[92:95], v[124:127], v[220:223], v[92:95]\n\t"
  "s_waitcnt vmcnt(6)\n\t"
  "s_waitcnt lgkmcnt(3)\n\t"
  "v_mfma_f32_16x16x32_bf16 v[32:35], v[96:99], v[224:227], v[32:35]\n\t"
  "ds_read_b128 v[116:119], %[ac0] offset:8704\n\t"
  "v_mfma_f32_16x16x32_bf16 v[36:39], v[96:99], v[228:231], v[36:39]\n\t"
  "v_mfma_f32_16x16x32_bf16 v[40:43], v[100:103], v[224:227], v[40:43]\n\t"
  "ds_read_b128 v[120:123], %[ac0] offset:12288\n\t"
  "v_mfma_f32_16x16x32_bf16 v[44:47], v[100:103], v[228:231], v[44:47]\n\t"
  "s_waitcnt lgkmcnt(3)\n\t"
  "v_mfma_f32_16x16x32_bf16 v[48:51], v[104:107], v[224:227], v[48:51]\n\t"
  "ds_read_b128 v[124:127], %[ac0] offset:12800\n\t"
  "v_mfma_f32_16x16x32_bf16 v[52:55], v[104:107], v[228:231], v[52:55]\n\t"
  "v_mfma_f32_16x16x32_bf16 v[56:59], v[108:111], v[224:227], v[56:59]\n\t"
  "v_xor_b32 %[ac1], 0x18040, %[aw]\n\t"
  "ds_read_b128 v[96:99], %[ac1] offset:0\n\t"
  "v_mfma_f32_16x16x32_bf16 v[60:63], v[108:111], v[228:231], v[60:63]\n\t"
  "s_waitcnt lgkmcnt(3)\n\t"
  "v_mfma_f32_16x16x32_bf16 v[64:67], v[112:115], v[224:227], v[64:67]\n\t"
  "ds_read_b128 v[100:103], %[ac1] offset:512\n\t"
  "v_mfma_f32_16x16x32_bf16 v[68:71], v[112:115], v[228:231], v[68:71]\n\t"
  "v_mfma_f32_16x16x32_bf16 v[72:75], v[116:119], v[224:227], v[72:75]\n\t"
  "ds_read_b128 v[104:107], %[ac1] offset:4096\n\t"
  "v_mfma_f32_16x16x32_bf16 v[76:79], v[116:119], v[228:231], v[76:79]\n\t"
  "s_waitcnt lgkmcnt(3)\n\t"
  "v_mfma_f32_16x16x32_bf16 v[80:83], v[120:123], v[224:227], v[80:83]\n\t"
  "ds_read_b128 v[108:111], %[ac1] offset:4608\n\t"
  "v_mfma_f32_16x16x32_bf16 v[84:87], v[120:123], v[228:231], v[84:87]\n\t"
  "v_mfma_f32_16x16x32_bf16 v[88:91], v[124:127], v[224:227], v[88:91]\n\t"
  "ds_read_b128 v[112:115], %[ac1] offset:8192\n\t"
  "v_mfma_f32_16x16x32_bf16 v[92:95], v[124:127], v[228:231], v[92:95]\n\t"
  "s_waitcnt vmcnt(4)\n\t"
  "s_waitcnt lgkmcnt(3)\n\t"
  "v_mfma_f32_16x16x32_bf16 v[32:35], v[96:99], v[232:235], v[32:35]\n\t"
  "ds_read_b128 v[116:119], %[ac1] offset:8704\n\t"
  "v_mfma_f32_16x16x32_bf16 v[36:39], v[96:99], v[236:239], v[36:39]\n\t"
  "v_mfma_f32_16x16x32_bf16 v[40:43], v[100:103], v[232:235], v[40:43]\n\t"
  "ds_read_b128 v[120:123], %[ac1] offset:12288\n\t"
  "v_mfma_f32_16x16x32_bf16 v[44:47], v[100:103], v[236:239], v[44:47]\n\t"
  "s_waitcnt lgkmcnt(3)\n\t"
  "v_mfma_f32_16x16x32_bf16 v[48:51], v[104:107], v[232:235], v[48:51]\n\t"
  "ds_read_b128 v[124:127], %[ac1] offset:12800\n\t"
  "v_mfma_f32_16x16x32_bf16 v[52:55], v[104:107], v[236:239], v[52:55]\n\t"
  "v_mfma_f32_16x16x32_bf16 v[56:59], v[108:111], v[232:235], v[56:59]\n\t"
  "s_barrier\n\t"
  "v_xor_b32 %[ac0], 0x20000, %[aw]\n\t"
  "ds_read_b128 v[96:99], %[ac0] offset:0\n\t"
  "v_mfma_f32_16x16x32_bf16 v[60:63], v[108:111], v[236:239], v[60:63]\n\t"
  "s_waitcnt lgkmcnt(3)\n\t"
  "v_mfma_f32_16x16x32_bf16 v[64:67], v[112:115], v[232:235], v[64:67]\n\t"
  "ds_read_b128 v[100:103], %[ac0] offset:512\n\t"
  "v_mfma_f32_16x16x32_bf16 v[68:71], v[112:115], v[236:239], v[68:71]\n\t"
  "v_mfma_f32_16x16x32_bf16 v[72:75], v[116:119], v[232:235], v[72:75]\n\t"
  "ds_read_b128 v[104:107], %[ac0] offset:4096\n\t"
  "v_mfma_f32_16x16x32_bf16 v[76:79], v[116:119], v[236:239], v[76:79]\n\t"
  "s_waitcnt lgkmcnt(3)\n\t"
  "v_mfma_f32_16x16x32_bf16 v[80:83], v[120:123], v[232:235], v[80:83]\n\t"
  "ds_read_b128 v[108:111], %[ac0] offset:4608\n\t"
  "v_mfma_f32_16x16x32_bf16 v[84:87], v[120:123], v[236:239], v[84:87]\n\t"
  "v_mfma_f32_16x16x32_bf16 v[88:91], v[124:127], v[232:235], v[88:91]\n\t"
  "ds_read_b128 v[112:115], %[ac0] offset:8192\n\t"
  "v_mfma_f32_16x16x32_bf16 v[92:95], v[124:127], v[236:239], v[92:95]\n\t"
  "s_waitcnt vmcnt(2)\n\t"
  "s_waitcnt lgkmcnt(3)\n\t"
  "v_mfma_f32_16x16x32_bf16 v[32:35], v[96:99], v[240:243], v[32:35]\n\t"
  "ds_read_b128 v[116:119], %[ac0] offset:8704\n\t"
  "v_mfma_f32_16x16x32_bf16 v[36:39], v[96:99], v[244:247], v[36:39]\n\t"
  "v_mfma_f32_16x16x32_bf16 v[40:43], v[100:103], v[240:243], v[40:43]\n\t"
  "ds_read_b128 v[120:123], %[ac0] offset:12288\n\t"
  "v_mfma_f32_16x16x32_bf16 v[44:47], v[100:103], v[244:247], v[44:47]\n\t"
  "s_waitcnt lgkmcnt(3)\n\t"
  "v_mfma_f32_16x16x32_bf16 v[48:51], v[104:107], v[240:243], v[48:51]\n\t"
  "ds_read_b128 v[124:127], %[ac0] offset:12800\n\t"
  "v_mfma_f32_16x16x32_bf16 v[52:55], v[104:107], v[244:247], v[52:55]\n\t"
  "v_mfma_f32_16x16x32_bf16 v[56:59], v[108:111], v[240:243], v[56:59]\n\t"
  "v_xor_b32 %[ac1], 0x20040, %[aw]\n\t"
  "ds_read_b128 v[96:99], %[ac1] offset:0\n\t"
  "v_mfma_f32_16x16x32_bf16 v[60:63], v[108:111], v[244:247], v[60:63]\n\t"
  "s_waitcnt lgkmcnt(3)\n\t"
  "v_mfma_f32_16x16x32_bf16 v[64:67], v[112:115], v[240:243], v[64:67]\n\t"
  "ds_read_b128 v[100:103], %[ac1] offset:512\n\t"
  "v_mfma_f32_16x16x32_bf16 v[68:71], v[112:115], v[244:247], v[68:71]\n\t"
  "v_mfma_f32_16x16x32_bf16 v[72:75], v[116:119], v[240:243], v[72:75]\n\t"
  "ds_read_b128 v[104:107], %[ac1] offset:4096\n\t"
  "v_mfma_f32_16x16x32_bf16 v[76:79], v[116:119], v[244:247], v[76:79]\n\t"
  "s_waitcnt lgkmcnt(3)\n\t"
  "v_mfma_f32_16x16x32_bf16 v[80:83], v[120:123], v[240:243], v[80:83]\n\t"
  "ds_read_b128 v[108:111], %[ac1] offset:4608\n\t"
  "v_mfma_f32_16x16x32_bf16 v[84:87], v[120:123], v[244:247], v[84:87]\n\t"
  "v_mfma_f32_16x16x32_bf16 v[88:91], v[124:127], v[240:243], v[88:91]\n\t"
  "ds_read_b128 v[112:115], %[ac1] offset:8192\n\t"
  "v_mfma_f32_16x16x32_bf16 v[92:95], v[124:127], v[244:247], v[92:95]\n\t"
  "s_waitcnt vmcnt(0)\n\t"
  "s_waitcnt lgkmcnt(3)\n\t"
  "v_mfma_f32_16x16x32_bf16 v[32:35], v[96:99], v[248:251], v[32:35]\n\t"
  "ds_read_b128 v[116:119], %[ac1] offset:8704\n\t"
  "v_mfma_f32_16x16x32_bf16 v[36:39], v[96:99], v[252:255], v[36:39]\n\t"
  "v_mfma_f32_16x16x32_bf16 v[40:43], v[100:103], v[248:251], v[40:43]\n\t"
  "ds_read_b128 v[120:123], %[ac1] offset:12288\n\t"
  "v_mfma_f32_16x16x32_bf16 v[44:47], v[100:103], v[252:255], v[44:47]\n\t"
  "s_waitcnt lgkmcnt(3)\n\t"
  "v_mfma_f32_16x16x32_bf16 v[48:51], v[104:107], v[248:251], v[48:51]\n\t"
  "ds_read_b128 v[124:127], %[ac1] offset:12800\n\t"
  "v_mfma_f32_16x16x32_bf16 v[52:55], v[104:107], v[252:255], v[52:55]\n\t"
  "v_mfma_f32_16x16x32_bf16 v[56:59], v[108:111], v[248:251], v[56:59]\n\t"
  "v_mfma_f32_16x16x32_bf16 v[60:63], v[108:111], v[252:255], v[60:63]\n\t"
  "s_waitcnt lgkmcnt(2)\n\t"
  "v_mfma_f32_16x16x32_bf16 v[64:67], v[112:115], v[248:251], v[64:67]\n\t"
  "v_mfma_f32_16x16x32_bf16 v[68:71], v[112:115], v[252:255], v[68:71]\n\t"
  "v_mfma_f32_16x16x32_bf16 v[72:75], v[116:119], v[248:251], v[72:75]\n\t"
  "v_mfma_f32_16x16x32_bf16 v[76:79], v[116:119], v[252:255], v[76:79]\n\t"
  "s_waitcnt lgkmcnt(0)\n\t"
  "v_mfma_f32_16x16x32_bf16 v[80:83], v[120:123], v[248:251], v[80:83]\n\t"
  "v_mfma_f32_16x16x32_bf16 v[84:87], v[120:123], v[252:255], v[84:87]\n\t"
  "v_mfma_f32_16x16x32_bf16 v[88:91], v[124:127], v[248:251], v[88:91]\n\t"
  "v_mfma_f32_16x16x32_bf16 v[92:95], v[124:127], v[252:255], v[92:95]\n\t"
  "s_nop 3\n\t"
  "v_xor_b32 %[ac0], 0x0, %[ao]\n\t"
  "v_cvt_pk_bf16_f32 v128, v32, v33\n\t"
  "v_cvt_pk_bf16_f32 v129, v34, v35\n\t"
  "v_cvt_pk_bf16_f32 v130, v40, v41\n\t"
  "v_cvt_pk_bf16_f32 v131, v42, v43\n\t"
  "ds_write_b128 %[ac0], v[128:131] offset:0\n\t"
  "v_cvt_pk_bf16_f32 v132, v36, v37\n\t"
  "v_cvt_pk_bf16_f32 v133, v38, v39\n\t"
  "v_cvt_pk_bf16_f32 v134, v44, v45\n\t"
  "v_cvt_pk_bf16_f32 v135, v46, v47\n\t"
  "ds_write_b128 %[ac0], v[132:135] offset:4096\n\t"
  "v_xor_b32 %[ac1], 0x40, %[ao]\n\t"
  "v_cvt_pk_bf16_f32 v136, v48, v49\n\t"
  "v_cvt_pk_bf16_f32 v137, v50, v51\n\t"
  "v_cvt_pk_bf16_f32 v138, v56, v57\n\t"
  "v_cvt_pk_bf16_f32 v139, v58, v59\n\t"
  "ds_write_b128 %[ac1], v[136:139] offset:0\n\t"
  "v_cvt_pk_bf16_f32 v140, v52, v53\n\t"
  "v_cvt_pk_bf16_f32 v141, v54, v55\n\t"
  "v_cvt_pk_bf16_f32 v142, v60, v61\n\t"
  "v_cvt_pk_bf16_f32 v143, v62, v63\n\t"
  "ds_write_b128 %[ac1], v[140:143] offset:4096\n\t"
  "v_xor_b32 %[ac0], 0x80, %[ao]\n\t"
  "v_cvt_pk_bf16_f32 v144, v64, v65\n\t"
  "v_cvt_pk_bf16_f32 v145, v66, v67\n\t"
  "v_cvt_pk_bf16_f32 v146, v72, v73\n\t"
  "v_cvt_pk_bf16_f32 v147, v74, v75\n\t"
  "ds_write_b128 %[ac0], v[144:147] offset:0\n\t"
  "v_cvt_pk_bf16_f32 v148, v68, v69\n\t"
  "v_cvt_pk_bf16_f32 v149, v70, v71\n\t"
  "v_cvt_pk_bf16_f32 v150, v76, v77\n\t"
  "v_cvt_pk_bf16_f32 v151, v78, v79\n\t"
  "ds_write_b128 %[ac0], v[148:151] offset:4096\n\t"
  "v_xor_b32 %[ac1], 0xc0, %[ao]\n\t"
  "v_cvt_pk_bf16_f32 v152, v80, v81\n\t"
  "v_cvt_pk_bf16_f32 v153, v82, v83\n\t"
  "v_cvt_pk_bf16_f32 v154, v88, v89\n\t"
  "v_cvt_pk_bf16_f32 v155, v90, v91\n\t"
  "ds_write_b128 %[ac1], v[152:155] offset:0\n\t"
  "v_cvt_pk_bf16_f32 v156, v84, v85\n\t"
  "v_cvt_pk_bf16_f32 v157, v86, v87\n\t"
  "v_cvt_pk_bf16_f32 v158, v92, v93\n\t"
  "v_cvt_pk_bf16_f32 v159, v94, v95\n\t"
  "ds_write_b128 %[ac1], v[156:159] offset:4096\n\t"
  "s_waitcnt lgkmcnt(0)\n\t"
  "s_barrier\n\t"
  : [ac0]"=&v"(ac[0]),[ac1]"=&v"(ac[1]),"+{v[32:47]}"(y0),"+{v[48:63]}"(y1),"+{v[64:79]}"(y2),"+{v[80:95]}"(y3)
  : [vl]"v"(vl),[aw]"v"(aw),[ao]"v"(ao),[pb0]"s"(pb[0]),[pb1]"s"(pb[1]),[pb2]"s"(pb[2]),[pb3]"s"(pb[3])
  : "memory","scc","vcc","v96","v97","v98","v99","v100","v101","v102","v103","v104","v105","v106","v107","v108","v109","v110","v111","v112","v113","v114","v115","v116","v117","v118","v119","v120","v121","v122","v123","v124","v125","v126","v127","v128","v129","v130","v131","v132","v133","v134","v135","v136","v137","v138","v139","v140","v141","v142","v143","v144","v145","v146","v147","v148","v149","v150","v151","v152","v153","v154","v155","v156","v157","v158","v159","v160","v161","v162","v163","v164","v165","v166","v167","v168","v169","v170","v171","v172","v173","v174","v175","v176","v177","v178","v179","v180","v181","v182","v183","v184","v185","v186","v187","v188","v189","v190","v191","v192","v193","v194","v195","v196","v197","v198","v199","v200","v201","v202","v203","v204","v205","v206","v207","v208","v209","v210","v211","v212","v213","v214","v215","v216","v217","v218","v219","v220","v221","v222","v223","v224","v225","v226","v227","v228","v229","v230","v231","v232","v233","v234","v235","v236","v237","v238","v239","v240","v241","v242","v243","v244","v245","v246","v247","v248","v249","v250","v251","v252","v253","v254","v255");

      finish();
      asm volatile("s_nop 1":::"memory");
    }
  } else {
    const int hl=loc-20;
    const int cid=xcd*10+hl, cc=cid>>3, cb=cid&7;
    auto lineaddr=[&](int g,unsigned& st)->const char*{
      bool isA=g<512; st= isA? 128u : 81920u;
      return isA? (const char*)((g&64)?Ci:Cr)+(size_t)(xcd*256+(g>>7)*64+(g&63))*4096 : (const char*)x+(g-512)*128; };
    auto wrow=[&](int e)->const char*{ return e<640? (const char*)Wr+e*1280 : (const char*)Wi+(e-640)*1280; };
    if(hl>=10){
      if(wid==7){
        const unsigned tstart=(unsigned)__builtin_amdgcn_s_memrealtime();
        const char* pa[9]; unsigned st[9];
        #pragma unroll
        for(int u=0;u<9;++u) pa[u]=lineaddr((u*2+hl-10)*64+lane,st[u]);
        #pragma unroll 1
        for(int s=0;s<32;++s){
          if(!((PURE_MASK>>s)&1)) continue;
          unsigned ks=(s+rot)&31;
          #pragma unroll
          for(int u=0;u<9;++u) if(u<4 || !((XSKIP>>s)&1)) pf(pa[u]+ks*st[u]);
        }
        const char* w0=wrow((hl-10)*640+lane);
        #pragma unroll 1
        for(int q=0;q<W_PURE;++q){
          #pragma unroll 1
          for(int u=0;u<10;++u){ int uu=u+xcd; if(uu>=10) uu-=10; pf(w0+uu*64*1280+wcol(uu>>1,q)); }
        }
        if(blockIdx.x==255){
          u64 tag=sld8(ws+WS_EP)+1;
          while((unsigned)__builtin_amdgcn_s_memrealtime()-tstart<EP_AT) __builtin_amdgcn_s_sleep(8);
          unsigned vo[4];
          #pragma unroll
          for(int g=0;g<4;++g){ int id=g*64+lane; if(id>=240) id=0;
            vo[g]= id<160? (unsigned)(WS_FA+(id/5)*8192+(id%5)*9*128) : (unsigned)(WS_FB+((id-160)>>3)*8192+((id-160)&7)*9*128); }
          #pragma unroll 1
          for(int n=0;n<SPIN;++n){
            u64 f0,f1,f2,f3;
            asm volatile("global_load_dwordx2 %0, %4, %8 sc1\n\tglobal_load_dwordx2 %1, %5, %8 sc1\n\tglobal_load_dwordx2 %2, %6, %8 sc1\n\tglobal_load_dwordx2 %3, %7, %8 sc1\n\ts_waitcnt vmcnt(0)"
              :"=&v"(f0),"=&v"(f1),"=&v"(f2),"=&v"(f3):"v"(vo[0]),"v"(vo[1]),"v"(vo[2]),"v"(vo[3]),"s"(ws):"memory");
            if(__builtin_amdgcn_ballot_w64(f0!=tag || f1!=tag || f2!=tag || f3!=tag)==0) break;
            __builtin_amdgcn_s_sleep(8);
          }
          if(lane==0) __hip_atomic_store((u64*)(ws+WS_EP),tag,__ATOMIC_RELAXED,__HIP_MEMORY_SCOPE_AGENT);
        }
      }
    } else {
      const char* pa[2]; unsigned st[1];
      auto run=[&](int s0,int s1){
        #pragma unroll 1
        for(int s=s0;s<s1;++s){
          if(((PURE_MASK|SKIP_MASK)>>s)&1) continue;
          unsigned ks=(s+rot)&31;
          pf(pa[0]+ks*128u); if(!((XSKIP>>s)&1)) pf(pa[1]+ks*81920u);
        } };
      const int T0=cb*256;
      u32x2 wq;
      asm volatile("global_load_dwordx2 %0, %1, %2":"=&v"(wq):"v"((cc*64+lane)*8),"s"(sk):"memory");
      if(wid==7){
        { int g=hl*52+(lane<52?lane:51); pa[0]=lineaddr(g<512?g:511,st[0]); pa[1]=(const char*)x+(hl*64+lane)*128; }
        run(0,S_KV);
      }
      const u64 tag=sld8(ws+WS_EP)+1;
      const unsigned dd=(unsigned)(sld8((const char*)Dv+cid*16+(wid>>2)*8)>>(16*(wid&3)))&0xffffu;
      u32x4 hw[4];
      const int ch=cid*8+wid;
      {
        const unsigned vo=(lane>>3)*1280+(((lane&7)^((lane>>2)&4))<<4);
        const unsigned v4[4]={vo,vo+10240-1024,vo+20480-2048,vo+30720-3072};
        const char* kb=(const char*)kg+(size_t)cc*128+(size_t)(T0+wid*32)*1280; const char* vb=(const char*)vg+(size_t)cc*128+(size_t)(T0+wid*32)*1280;
        if(wid==7) wait_vm<PF_DEPTH>();
        bar();
        if(wid==0) glds1(0,vo,cb? kb-8*1280 : kb);
        glds4(1024+wid*4096,v4,kb); glds4(VOFF+wid*4096,v4,vb);
        bar();
        asm volatile("global_load_dwordx4 %0, %4, %5\n\tglobal_load_dwordx4 %1, %4, %5 offset:1024\n\tglobal_load_dwordx4 %2, %4, %5 offset:2048\n\tglobal_load_dwordx4 %3, %4, %5 offset:3072"
          :"=&v"(hw[0]),"=&v"(hw[1]),"=&v"(hw[2]),"=&v"(hw[3]):"v"(lane*16),"s"((const char*)hk+(size_t)ch*4096):"memory");
        if(wid==7){ __builtin_amdgcn_s_sleep(2); run(S_KV,S_ST); wait_vm<4+npf(S_KV,S_ST)>(); }
        else wait_vm<4>();
      }
      asm volatile("s_barrier":"+v"(wq)::"memory");
      {
        u32x2 kq[9], vq[8];
        const unsigned ta=(((lane>>2)&3)*128+(((lane>>4)*32+(lane&3)*8)^((lane&8)<<3)))+wid*4096;
        asm volatile("ds_read_b64_tr_b16 %0, %17 offset:512\n\tds_read_b64_tr_b16 %1, %17 offset:1024\n\tds_read_b64_tr_b16 %2, %17 offset:1536\n\t"
                     "ds_read_b64_tr_b16 %9, %17 offset:%18\n\tds_read_b64_tr_b16 %10, %17 offset:%18+512\n\t"
                     "ds_read_b64_tr_b16 %3, %17 offset:2048\n\tds_read_b64_tr_b16 %4, %17 offset:2560\n\t"
                     "ds_read_b64_tr_b16 %11, %17 offset:%18+1024\n\tds_read_b64_tr_b16 %12, %17 offset:%18+1536\n\t"
                     "ds_read_b64_tr_b16 %5, %17 offset:3072\n\tds_read_b64_tr_b16 %6, %17 offset:3584\n\t"
                     "ds_read_b64_tr_b16 %13, %17 offset:%18+2048\n\tds_read_b64_tr_b16 %14, %17 offset:%18+2560\n\t"
                     "ds_read_b64_tr_b16 %7, %17 offset:4096\n\tds_read_b64_tr_b16 %8, %17 offset:4608\n\t"
                     "ds_read_b64_tr_b16 %15, %17 offset:%18+3072\n\tds_read_b64_tr_b16 %16, %17 offset:%18+3584\n\ts_waitcnt lgkmcnt(0)"
          :"=&v"(kq[0]),"=&v"(kq[1]),"=&v"(kq[2]),"=&v"(kq[3]),"=&v"(kq[4]),"=&v"(kq[5]),"=&v"(kq[6]),"=&v"(kq[7]),"=&v"(kq[8]),
           "=&v"(vq[0]),"=&v"(vq[1]),"=&v"(vq[2]),"=&v"(vq[3]),"=&v"(vq[4]),"=&v"(vq[5]),"=&v"(vq[6]),"=&v"(vq[7]):"v"(ta),"i"(VOFF):"memory");
        if(cb==0 && wid==0) kq[0]=u32x2{0,0};
        const float w0=bflo(wq[0]), w1=bfhi(wq[0]), w2=bflo(wq[1]), w3=bfhi(wq[1]);
        const char* xb=ws+WS_R+(size_t)cc*262144+(255-cb*32-wid*4)*128; const unsigned xo=(lane>>3)*32768+(lane&7)*16;
        #pragma unroll
        for(int c=0;c<4;++c){
          u32x4 o;
          #pragma unroll
          for(int mm=0;mm<4;++mm){
            const int m=c*4+mm;
            const unsigned d0=kq[m>>1][m&1], d1=kq[(m+1)>>1][(m+1)&1], d2=kq[(m+2)>>1][(m+2)&1], vv=vq[m>>1][m&1];
            const float ae=fma1(bflo(d2),w0,fma1(bfhi(d1),w1,fma1(bflo(d1),w2,mul1(bfhi(d0),w3))));
            const float ao=fma1(bfhi(d2),w0,fma1(bflo(d2),w1,fma1(bfhi(d1),w2,mul1(bflo(d1),w3))));
            o[3-mm]=cvtpk(mul1(ao,bfhi(vv)),mul1(ae,bflo(vv)));
          }
          st16_dev(xo,xb-c*128,o);
        }
      }
      wait_vm<0>();
      asm volatile("s_barrier":"+v"(hw[0]),"+v"(hw[1]),"+v"(hw[2]),"+v"(hw[3])::"memory");
      const char* fl=ws+WS_FB+(size_t)cc*8192;
      if(wid==4) st16_dev(((lane>>3)*8+cb)*128+(lane&7)*16,fl,tag4(tag));
      if(wid==7){ __builtin_amdgcn_s_sleep(1); run(S_ST,S_U); }
      Shift sh[4];
      #pragma unroll
      for(int r=0;r<4;++r){
        const u32x4 w=hw[r]; unsigned pv[2];
        #pragma unroll
        for(int e=0;e<2;++e) pv[e]=(unsigned)__builtin_amdgcn_update_dpp(r? __builtin_amdgcn_readlane((int)hw[r?r-1:0][2+e],63) : 0,(int)w[2+e],0x138,0xf,0xf,false);
        const unsigned om2=__builtin_amdgcn_alignbit(pv[1],pv[0],16), om1=__builtin_amdgcn_alignbit(w[0],pv[1],16);
        const unsigned o0=__builtin_amdgcn_alignbit(w[1],w[0],16), o1=__builtin_amdgcn_alignbit(w[2],w[1],16), o2=__builtin_amdgcn_alignbit(w[3],w[2],16);
        sh[r].a=u32x4{om2,om1,o0,o1}; sh[r].b=u32x4{pv[1],w[0],w[1],w[2]}; sh[r].c=u32x4{om1,o0,o1,o2};
      }
      if(wid<4){ fill_filter(sh,hw,smem+wid*HIM,lane); if(lane<12) *(u32x4*)(smem+wid*HIM+(lane&3)*HCS+(lane>>2)*16)=u32x4{0,0,0,0}; }
      { char* Lz=smem+ROFF+wid*RIM; if(lane<60) *(u32x2*)(Lz+4096+lane*8)=u32x2{0,0}; if(lane<59) *(u32x2*)(Lz+RCS+4104+lane*8)=u32x2{0,0}; }
      if(wid==5){
        __builtin_amdgcn_s_sleep(F_DELAY);
        #pragma unroll 1
        for(int n=0;n<SPIN;++n){ if(__builtin_amdgcn_ballot_w64(ld8_dev((cb*8+(lane&7))*128,fl)!=tag)==0) break; __builtin_amdgcn_s_sleep(1); }
      }
      bar();
      {
        u32x4 uw[4]; const char* rp=ws+WS_R+(size_t)cid*32768+wid*4096;
        asm volatile("global_load_dwordx4 %0, %4, %5 sc1\n\tglobal_load_dwordx4 %1, %4, %5 offset:1024 sc1\n\tglobal_load_dwordx4 %2, %4, %5 offset:2048 sc1\n\tglobal_load_dwordx4 %3, %4, %5 offset:3072 sc1"
          :"=&v"(uw[0]),"=&v"(uw[1]),"=&v"(uw[2]),"=&v"(uw[3]):"v"(lane*16),"s"(rp):"memory");
        if(wid==7){ __builtin_amdgcn_s_sleep(2); run(S_U,S_V); wait_vm<npf(S_U,S_V)>(); } else wait_vm<0>();
        asm volatile("":"+v"(uw[0]),"+v"(uw[1]),"+v"(uw[2]),"+v"(uw[3]));
        char* Lr=smem+ROFF+(lane&7)*RIM+(wid*32+(lane>>3))*16;
        #pragma unroll
        for(int r=0;r<4;++r){ const u32x4 w=uw[r]; *(u32x4*)(Lr+r*128)=w; *(u32x2*)(Lr+RCS+r*128+8)=u32x2{w[0],w[1]}; *(u32x2*)(Lr+RCS+r*128+16)=u32x2{w[2],w[3]}; }
        asm volatile("s_waitcnt lgkmcnt(0)\n\ts_barrier":::"memory");
      }
      const unsigned trow=256*wid+128*(cb&1)+8*(cb>>1)+(lane>>3);
      const unsigned t32=(unsigned)tag<<7;
      if(wid>=4){
        if(wid==7){
          run(S_V,32);
          int hr=hl+xcd; if(hr>=10) hr-=10;
          const char* w0=wrow(hr*128+lane); const char* w1=w0+64*1280;
          #pragma unroll 1
          for(int q=W_PURE;q<10;++q){ const int col=wcol(hr>=5?hr-5:hr,q); pf(w0+col); pf(w1+col); }
        }
        bar();
        fill_filter(sh,hw,smem+(wid-4)*HIM,lane);
      }
      asm volatile("s_waitcnt lgkmcnt(0)":::"memory");
      u32x4 qq[4];
      {
        unsigned y[16];
        conv_chan(y,(wid&3)*HIM,ROFF+wid*RIM,dd,lane);
        if(wid<4){ bar(); }
        put_y(y,ws+WS_Y+(size_t)(cc*512+(ch&63))*640,t32,lane);
      }
      if(wid<4) pf((const char*)qg+cc*128+(size_t)(256*(2*wid+(lane>>5))+128*(cb&1)+8*(cb>>1)+32*((lane>>3)&3)+(lane&7))*1280);
      bar();
      { const char* qb=(const char*)qg+cc*128; const unsigned qo=trow*1280+(lane&7)*16;
        asm volatile("global_load_dwordx4 %0, %4, %5\n\tglobal_load_dwordx4 %1, %4, %6\n\tglobal_load_dwordx4 %2, %4, %7\n\tglobal_load_dwordx4 %3, %4, %8"
          :"=&v"(qq[0]),"=&v"(qq[1]),"=&v"(qq[2]),"=&v"(qq[3]):"v"(qo),"s"(qb),"s"(qb+40960),"s"(qb+81920),"s"(qb+122880):"memory"); }
      asm volatile("s_waitcnt vmcnt(0)":"+v"(qq[0]),"+v"(qq[1]),"+v"(qq[2]),"+v"(qq[3])::"memory");
      __builtin_amdgcn_s_sleep(Y_DELAY);
      {
        u32x4 g[5]; const char* yp=ws+WS_Y+(size_t)(cid*64+wid*8)*640; const unsigned yo=(lane>>3)*640+(lane&7)*16;
        int n=0;
        #pragma unroll 1
        for(;n<SPIN;++n){
          asm volatile("global_load_dwordx4 %0, %5, %6 sc1\n\tglobal_load_dwordx4 %1, %5, %6 offset:128 sc1\n\tglobal_load_dwordx4 %2, %5, %6 offset:256 sc1\n\t"
                       "global_load_dwordx4 %3, %5, %6 offset:384 sc1\n\tglobal_load_dwordx4 %4, %5, %6 offset:512 sc1\n\ts_waitcnt vmcnt(0)"
            :"=&v"(g[0]),"=&v"(g[1]),"=&v"(g[2]),"=&v"(g[3]),"=&v"(g[4]):"v"(yo),"s"(yp):"memory");
          const unsigned bad=(g[0][0]^t32)|(g[1][0]^t32)|(g[2][0]^t32)|(g[3][0]^t32)|(g[4][0]^t32);
          if(__builtin_amdgcn_ballot_w64(bad>127u)==0) break;
          __builtin_amdgcn_s_sleep(8);
        }

        const unsigned w15=(g[0][0]&127u)|((g[1][0]&127u)<<7)|((g[2][0]&127u)<<14)|((g[3][0]&127u)<<21)|(g[4][0]<<28);
        char* y1=smem+(wid*8+(lane>>3))*576+(lane&7)*8;
        #pragma unroll
        for(int n2=0;n2<8;++n2){ const int d=2*n2; *(u32x2*)(y1+64*n2)=u32x2{g[d/3][1+d%3], d<14? g[d<14?(d+1)/3:0][1+(d+1)%3] : w15}; }
        u32x2 a[4],b[4]; const unsigned ya=(wid*8+((lane>>2)&3))*576+(lane>>4)*32+(lane&3)*8;
        asm volatile("s_waitcnt lgkmcnt(0)\n\tds_read_b64_tr_b16 %0, %8\n\tds_read_b64_tr_b16 %4, %8 offset:2304\n\tds_read_b64_tr_b16 %1, %8 offset:128\n\tds_read_b64_tr_b16 %5, %8 offset:2432\n\t"
                     "ds_read_b64_tr_b16 %2, %8 offset:256\n\tds_read_b64_tr_b16 %6, %8 offset:2560\n\tds_read_b64_tr_b16 %3, %8 offset:384\n\tds_read_b64_tr_b16 %7, %8 offset:2688\n\ts_waitcnt lgkmcnt(0)"
          :"=&v"(a[0]),"=&v"(a[1]),"=&v"(a[2]),"=&v"(a[3]),"=&v"(b[0]),"=&v"(b[1]),"=&v"(b[2]),"=&v"(b[3]):"v"(ya):"memory");
        char* y2=smem+40960;
        #pragma unroll
        for(int k=0;k<4;++k) *(u32x4*)(y2+(k*64+lane)*128+((wid^(lane&7))<<4))=u32x4{a[k][0],a[k][1],b[k][0],b[k][1]};
        asm volatile("s_waitcnt lgkmcnt(0)\n\ts_barrier":::"memory");
        #pragma unroll
        for(int k=0;k<4;++k){
          const u32x4 y=*(const u32x4*)(y2+(wid*32+k*8+(lane>>3))*128+(((lane&7)^(lane>>3))<<4)), q=qq[k]; u32x4 o;
          #pragma unroll
          for(int e=0;e<4;++e) o[e]=cvtpk(bflo(y[e])*bflo(q[e]),bfhi(y[e])*bfhi(q[e]));
          st_out(out+(size_t)(trow+32*k)*2*D+D+cc*64+(lane&7)*8,o);
        }
      }
    }
  }
}

extern "C" void kernel_launch(void* const* d_in, const int* in_sizes, int n_in, void* d_out, int out_size, void* d_ws, size_t ws_size, hipStream_t stream){
  (void)hipFuncSetAttribute((const void*)fused,hipFuncAttributeMaxDynamicSharedMemorySize,LDSB);
  fused<<<256,512,LDSB,stream>>>((const u16*)d_in[0],(const u16*)d_in[1],(const u16*)d_in[2],(char*)d_ws,(const u16*)d_in[3],(const u16*)d_in[4],
     (u16*)d_out,(const u16*)d_in[8],(const u16*)d_in[5],(const u16*)d_in[6],(const u16*)d_in[7],(const u16*)d_in[9],(const u16*)d_in[10]);
}
```
